```python
import torch

def _softmax(x, axis=-1):
    return torch.softmax(x, dim=axis)
def _gelu(x):
    return torch.nn.functional.gelu(x, approximate="tanh")
def _silu(x):
    return torch.nn.functional.silu(x)
def _sigmoid(x):
    return torch.sigmoid(x)
def _layernorm(x, axis=-1, eps=1e-5):
    m = x.mean(dim=axis, keepdim=True)
    v = x.var(dim=axis, keepdim=True, unbiased=False)
    return (x - m) / torch.sqrt(v + eps)
def _scatter_add(tgt, idx, src, axis=0):
    return tgt.clone().index_add_(axis, idx, src)
def _segment_sum(src, seg, n):
    out = torch.zeros((n,) + src.shape[1:], dtype=src.dtype)
    return out.index_add_(0, seg, src)
def _pad(x, pw):
    flat = tuple(v for pair in reversed(pw) for v in pair)
    return torch.nn.functional.pad(x, flat)

kernel_name = 'recurrent_scan__ssm_conv_s4d__dense_activation_squared_relu__prompt'

def setup_inputs():

    _w = (lambda a: (lambda t: t.to(torch.bfloat16) if t.is_floating_point() else t)(torch.as_tensor(a)))
    def _A_setup():
        Bsz, L, D, N = 1, 2048, 448, 256
        return {
            "u": _w((lambda *s: torch.randn(*s, dtype=torch.bfloat16) * 0.05)(Bsz, L, D)),
            "log_A_real": _w((lambda *s: torch.randn(*s, dtype=torch.bfloat16) * 3.2)(D, N) * 0.5),
            "A_imag": _w((lambda *s: torch.randn(*s, dtype=torch.bfloat16) * 0.05)(D, N) * 3.14),
            "C_real": _w((lambda *s: torch.randn(*s, dtype=torch.bfloat16) * 0.2)(D, N) / N ** 0.5),
            "C_imag": _w((lambda *s: torch.randn(*s, dtype=torch.bfloat16) * 0.2)(D, N) / N ** 0.5),
            "log_dt": _w((lambda *s: torch.randn(*s, dtype=torch.bfloat16) * 0.05)(D) * 0.5 - 4.0),
            "D_skip": _w((lambda *s: torch.randn(*s, dtype=torch.bfloat16) * 0.05)(D)),
        }
    _A_in = _A_setup()

    def _B_setup():
        B, S, D, F = 1, 2048, 448, 1792
        return {
            "x": _w((lambda *s: torch.randn(*s, dtype=torch.bfloat16) * 0.05)(B, S, D)),
            "w1": _w((lambda *s: torch.randn(*s, dtype=torch.bfloat16) * 0.05)(D, F)),
            "b1": _w((lambda *s: torch.randn(*s, dtype=torch.bfloat16) * 0.4)(F)),
            "w2": _w((lambda *s: torch.randn(*s, dtype=torch.bfloat16) * 0.05)(F, D)),
            "b2": _w((lambda *s: torch.randn(*s, dtype=torch.bfloat16) * 0.8)(D)),
        }
    _B_in = _B_setup()

    return {
        "A_u": _A_in["u"],
        "A_log_A_real": _A_in["log_A_real"],
        "A_A_imag": _A_in["A_imag"],
        "A_C_real": _A_in["C_real"],
        "A_C_imag": _A_in["C_imag"],
        "A_log_dt": _A_in["log_dt"],
        "A_D_skip": _A_in["D_skip"],
        "B_x": _B_in["x"],
        "B_w1": _B_in["w1"],
        "B_b1": _B_in["b1"],
        "B_w2": _B_in["w2"],
        "B_b2": _B_in["b2"],
    }

def reference(A_u, A_log_A_real, A_A_imag, A_C_real, A_C_imag, A_log_dt, A_D_skip, B_x, B_w1, B_b1, B_w2, B_b2):
    def _A_ref(u, log_A_real, A_imag, C_real, C_imag, log_dt, D_skip):
        L = u.shape[1]
        dt = torch.exp(log_dt)
        A = -torch.exp(log_A_real) + 1j * A_imag
        C = (C_real + 1j * C_imag) * dt[:, None]
        pos = (lambda x, dt: x.to(dt))(torch.arange(L), log_dt.dtype)
        P = torch.exp((A * dt[:, None])[:, :, None] * pos[None, None, :])
        K = 2.0 * torch.einsum("dn,dnl->dl", C, P).real
        L2 = 2 * L
        u_t = (lambda x, *d: x.permute(*d))(u, 0, 2, 1)
        Kf = (lambda x, *a, **k: torch.fft.rfft(x.float(), *a, **k))(K, L2)
        Uf = (lambda x, *a, **k: torch.fft.rfft(x.float(), *a, **k))(u_t, L2)
        y = (lambda x, *a, **k: torch.fft.irfft(x, *a, **k).to(torch.bfloat16))(Uf * Kf[None, :, :], L2)[:, :, :L]
        y = (lambda x, *d: x.permute(*d))(y, 0, 2, 1)
        return y + u * D_skip[None, None, :]

    def _B_ref(x, w1, b1, w2, b2):
        h = torch.einsum("bsd,df->bsf", x, w1) + b1
        zero = torch.zeros_like(h)
        r = torch.where(h > zero, h, zero)
        h = r * r
        out = torch.einsum("bsf,fd->bsd", h, w2) + b2
        return out

    x = A_u
    xn = _layernorm(x, axis=-1)
    s = _A_ref(xn, A_log_A_real, A_A_imag, A_C_real, A_C_imag, A_log_dt, A_D_skip)
    g = _silu(_B_ref(xn, B_w1, B_b1, B_w2, B_b2))
    return x + s * g
```

```cpp
#include <hip/hip_runtime.h>
#include <hip/hip_bf16.h>
#include <stdint.h>

typedef __attribute__((ext_vector_type(8))) short bf16x8;
typedef __attribute__((ext_vector_type(4))) float f32x4;
typedef __attribute__((ext_vector_type(16))) float f32x16;
typedef unsigned int u32;
typedef unsigned short u16;

#define LL 2048
#define DD 448
#define NN 256
#define FF 1792
#define KPAD 2304

#define WS_XN 0
#define WS_XNT (WS_XN + LL * DD * 2)
#define WS_W1T (WS_XNT + LL * DD * 2)
#define WS_W2T (WS_W1T + DD * FF * 2)
#define WS_KP (WS_W2T + DD * FF * 2)
#define WS_H (WS_KP + DD * 2 * KPAD * 2)
#define WS_ST (WS_H + LL * FF * 2)

__device__ __forceinline__ float bf2f(u16 v) { return __uint_as_float((u32)v << 16); }
__device__ __forceinline__ u16 f2bf(float f) { __bf16 b = (__bf16)f; return __builtin_bit_cast(u16, b); }
__device__ __forceinline__ float rbf(float f) { return (float)(__bf16)f; }
__device__ __forceinline__ float lo16f(u32 v) { return __uint_as_float(v << 16); }
__device__ __forceinline__ float hi16f(u32 v) { return __uint_as_float(v & 0xffff0000u); }
__device__ __forceinline__ u32 pack2(float a, float b) { return (u32)f2bf(a) | ((u32)f2bf(b) << 16); }

__device__ __forceinline__ float wave_sum(float v) {
#pragma unroll
  for (int o = 32; o > 0; o >>= 1) v += __shfl_xor(v, o);
  return v;
}

struct cplx { float re, im; };
__device__ __forceinline__ cplx cmul(cplx a, cplx b) {
  cplx r;
  r.re = fmaf(a.re, b.re, -a.im * b.im);
  r.im = fmaf(a.re, b.im, a.im * b.re);
  return r;
}
__device__ __forceinline__ cplx cexp_(float are, float aim) {
  float mag = __builtin_amdgcn_exp2f(are * 1.44269504088896340736f);
  const float I2P_HI = 0.15915494309189533577f;
  const float I2P_LO = 6.4206382e-09f;
  float t = aim * I2P_HI;
  float e = fmaf(aim, I2P_HI, -t) + aim * I2P_LO;
  float f = (t - rintf(t)) + e;
  cplx r;
  r.re = mag * __builtin_amdgcn_cosf(f);
  r.im = mag * __builtin_amdgcn_sinf(f);
  return r;
}

__device__ __forceinline__ void kg_store(char* tab, int idx, int n, float re, float im) {
  u32 hr = f2bf(re), hi = f2bf(im);
  float lr = re - bf2f((u16)hr), li = im - bf2f((u16)hi);
  u32 whi = hr | (hi << 16);
  u32 wlo = pack2(lr, li);
  int off = idx * 512 + ((((n >> 2) ^ (idx & 15))) << 4) + ((n & 3) << 2);
  *(u32*)(tab + off) = whi;
  *(u32*)(tab + 40 * 512 + off) = wlo;
}

__device__ void kgen_role(int d, const u16* __restrict__ logA, const u16* __restrict__ Aim,
                          const u16* __restrict__ Cre, const u16* __restrict__ Cim,
                          const u16* __restrict__ logdt, u16* __restrict__ Kp, char* smem) {
  const int tid = threadIdx.x, wave = tid >> 6, lane = tid & 63;
  const int role = tid >> 7;
  char* tabE = smem;
  char* tabG = smem + 2 * 40 * 512;
  char* tab = role ? tabG : tabE;
  const float dt = rbf(expf(bf2f(logdt[d])));
  f32x4 acc = {0.f, 0.f, 0.f, 0.f};
  float last = 0.f;

  const int seg = wave;
  const int fi = lane & 15, fq = lane >> 4;
  int gidx, eidx;
  if (seg == 0) { gidx = fi; eidx = fi; }
  else {
    gidx = 8 + 8 * seg + (fi & 7);
    int e = fi << seg;
    eidx = e < 16 ? e : (e < 32 ? 16 + ((e - 16) >> 1) : (e < 64 ? 24 + ((e - 32) >> 2) : 32 + ((e - 64) >> 3)));
  }

  for (int pass = 0; pass < 2; ++pass) {
    const int nl = tid & 127;
    const int n = pass * 128 + nl;
    {
      float ea = rbf(expf(bf2f(logA[d * NN + n])));
      float sc = role ? 16.f : 1.f;
      float are = (-ea * dt) * sc;
      float aim = (bf2f(Aim[d * NN + n]) * dt) * sc;
      cplx c = {1.f, 0.f};
      if (role) { c.re = bf2f(Cre[d * NN + n]) * dt; c.im = bf2f(Cim[d * NN + n]) * dt; }
      const float sg = role ? -1.f : 1.f;
#define PUT(IDX, V)                                        \
  {                                                        \
    cplx _t = role ? cmul(c, V) : V;                       \
    kg_store(tab, IDX, nl, _t.re, sg * _t.im);             \
  }
      cplx b0 = {1.f, 0.f};
      cplx b1 = cexp_(are, aim), b2 = cexp_(are * 2.f, aim * 2.f), b4 = cexp_(are * 4.f, aim * 4.f);
      cplx b8 = cexp_(are * 8.f, aim * 8.f), b16 = cexp_(are * 16.f, aim * 16.f);
      cplx b32 = cexp_(are * 32.f, aim * 32.f), b64 = cexp_(are * 64.f, aim * 64.f);
      cplx b3 = cmul(b2, b1), b5 = cmul(b4, b1), b6 = cmul(b4, b2), b7 = cmul(b4, b3);
      cplx b9 = cmul(b8, b1), b10 = cmul(b8, b2), b11 = cmul(b8, b3), b12 = cmul(b8, b4);
      cplx b13 = cmul(b8, b5), b14 = cmul(b8, b6), b15 = cmul(b8, b7);
      PUT(0, b0) PUT(1, b1) PUT(2, b2) PUT(3, b3) PUT(4, b4) PUT(5, b5) PUT(6, b6) PUT(7, b7)
      PUT(8, b8) PUT(9, b9) PUT(10, b10) PUT(11, b11) PUT(12, b12) PUT(13, b13) PUT(14, b14) PUT(15, b15)
      cplx b18 = cmul(b16, b2), b20 = cmul(b16, b4), b22 = cmul(b16, b6), b24 = cmul(b16, b8);
      cplx b26 = cmul(b16, b10), b28 = cmul(b16, b12), b30 = cmul(b16, b14);
      PUT(16, b16) PUT(17, b18) PUT(18, b20) PUT(19, b22) PUT(20, b24) PUT(21, b26) PUT(22, b28) PUT(23, b30)
      cplx b36 = cmul(b32, b4), b40 = cmul(b32, b8), b44 = cmul(b32, b12), b48 = cmul(b32, b16);
      cplx b52 = cmul(b32, b20), b56 = cmul(b32, b24), b60 = cmul(b32, b28);
      PUT(24, b32) PUT(25, b36) PUT(26, b40) PUT(27, b44) PUT(28, b48) PUT(29, b52) PUT(30, b56) PUT(31, b60)
      cplx b72 = cmul(b64, b8), b80 = cmul(b64, b16), b88 = cmul(b64, b24), b96 = cmul(b64, b32);
      cplx b104 = cmul(b64, b40), b112 = cmul(b64, b48), b120 = cmul(b64, b56);
      PUT(32, b64) PUT(33, b72) PUT(34, b80) PUT(35, b88) PUT(36, b96) PUT(37, b104) PUT(38, b112) PUT(39, b120)
#undef PUT
      if (role) {
        cplx b128 = cexp_(are * 128.f, aim * 128.f);
        last += cmul(c, b128).re;
      }
    }
    __syncthreads();
#pragma unroll
    for (int ks = 0; ks < 8; ++ks) {
      int chunk = ks * 4 + fq;
      int offG = gidx * 512 + ((chunk ^ (gidx & 15)) << 4);
      int offE = eidx * 512 + ((chunk ^ (eidx & 15)) << 4);
      bf16x8 gh = *(const bf16x8*)(tabG + offG);
      bf16x8 gl = *(const bf16x8*)(tabG + 40 * 512 + offG);
      bf16x8 eh = *(const bf16x8*)(tabE + offE);
      bf16x8 el = *(const bf16x8*)(tabE + 40 * 512 + offE);
      acc = __builtin_amdgcn_mfma_f32_16x16x32_bf16(gl, eh, acc, 0, 0, 0);
      acc = __builtin_amdgcn_mfma_f32_16x16x32_bf16(gh, el, acc, 0, 0, 0);
      acc = __builtin_amdgcn_mfma_f32_16x16x32_bf16(gh, eh, acc, 0, 0, 0);
    }
    __syncthreads();
  }
  float* Kd = (float*)smem;
  float* red = Kd + 1024;
  last = wave_sum(last);
  if (lane == 0) red[wave] = last;
#pragma unroll
  for (int r = 0; r < 4; ++r) {
    int row = fq * 4 + r;
    if (seg == 0) Kd[row * 16 + fi] = 2.f * acc[r];
    else if (row < 8) Kd[256 + (seg - 1) * 128 + row * 16 + fi] = 2.f * acc[r];
  }
  __syncthreads();
  if (tid == 0) Kd[640] = 2.f * (red[2] + red[3]);
  __syncthreads();
  u16* outh = Kp + (size_t)d * 2 * KPAD;
  u16* outl = outh + KPAD;
  {
    u32 wh[4], wl[4];
#pragma unroll
    for (int jj = 0; jj < 4; ++jj) {
      float kv[2];
#pragma unroll
      for (int e = 0; e < 2; ++e) {
        int l = 8 * tid + 7 - (2 * jj + e);
        int p = (int)rbf((float)l);
        int idx;
        if (p < 256) idx = p;
        else {
          int s = (31 - __clz(p)) - 7;
          idx = 256 + (s - 1) * 128 + ((p >> s) - 128);
        }
        kv[e] = Kd[idx];
      }
      u16 h0 = f2bf(kv[0]), h1 = f2bf(kv[1]);
      wh[jj] = (u32)h0 | ((u32)h1 << 16);
      wl[jj] = pack2(kv[0] - bf2f(h0), kv[1] - bf2f(h1));
    }
    *(uint4*)(outh + 256 + 8 * tid) = make_uint4(wh[0], wh[1], wh[2], wh[3]);
    *(uint4*)(outl + 256 + 8 * tid) = make_uint4(wl[0], wl[1], wl[2], wl[3]);
    if (tid < 32) {
      *(uint4*)(outh + 8 * tid) = make_uint4(0, 0, 0, 0);
      *(uint4*)(outl + 8 * tid) = make_uint4(0, 0, 0, 0);
    }
  }
}

__device__ void ln_role(int blk, const u16* __restrict__ x, u16* __restrict__ xn, u16* __restrict__ xnT, char* smem) {
  const int tid = threadIdx.x, wave = tid >> 6, lane = tid & 63;
  u16* tile = (u16*)smem;
  const bool act = lane < 56;
  uint4 raw[8];
#pragma unroll
  for (int i = 0; i < 8; ++i) {
    int l = blk * 32 + wave * 8 + i;
    raw[i] = act ? *(const uint4*)(x + (size_t)l * DD + lane * 8) : make_uint4(0, 0, 0, 0);
  }
#pragma unroll
  for (int i = 0; i < 8; ++i) {
    int row = wave * 8 + i;
    int l = blk * 32 + row;
    float v[8];
    v[0] = lo16f(raw[i].x); v[1] = hi16f(raw[i].x); v[2] = lo16f(raw[i].y); v[3] = hi16f(raw[i].y);
    v[4] = lo16f(raw[i].z); v[5] = hi16f(raw[i].z); v[6] = lo16f(raw[i].w); v[7] = hi16f(raw[i].w);
    float s = 0.f;
#pragma unroll
    for (int j = 0; j < 8; ++j) s += v[j];
    s = wave_sum(s);
    float mean = s * (1.f / DD);
    float q = 0.f;
#pragma unroll
    for (int j = 0; j < 8; ++j) { float t = v[j] - mean; q += t * t; }
    if (!act) q = 0.f;
    q = wave_sum(q);
    float var = q * (1.f / DD);
    float mb = rbf(mean), vb = rbf(var);
    float den = rbf(sqrtf(rbf(vb + 1e-5f)));
    u32 w[4];
#pragma unroll
    for (int j = 0; j < 4; ++j)
      w[j] = pack2(rbf(v[2 * j] - mb) / den, rbf(v[2 * j + 1] - mb) / den);
    if (act) {
      *(uint4*)(xn + (size_t)l * DD + lane * 8) = make_uint4(w[0], w[1], w[2], w[3]);
      u32* trow = (u32*)(tile + row * 450 + lane * 8);
      trow[0] = w[0]; trow[1] = w[1]; trow[2] = w[2]; trow[3] = w[3];
    }
  }
  __syncthreads();
  for (int d = tid; d < DD; d += 256) {
    u32 w[16];
#pragma unroll
    for (int r = 0; r < 16; ++r)
      w[r] = (u32)tile[(2 * r) * 450 + d] | ((u32)tile[(2 * r + 1) * 450 + d] << 16);
    uint4* dst = (uint4*)(xnT + (size_t)d * LL + blk * 32);
    dst[0] = make_uint4(w[0], w[1], w[2], w[3]);
    dst[1] = make_uint4(w[4], w[5], w[6], w[7]);
    dst[2] = make_uint4(w[8], w[9], w[10], w[11]);
    dst[3] = make_uint4(w[12], w[13], w[14], w[15]);
  }
}

__device__ void tr_role(const u16* __restrict__ in, u16* __restrict__ out, int R, int C, int tr, int tc, char* smem) {
  const int tid = threadIdx.x;
  u16* tile = (u16*)smem;
  __syncthreads();
#pragma unroll
  for (int i = 0; i < 2; ++i) {
    int v = tid + i * 256;
    int r = v >> 3, c8 = v & 7;
    uint4 raw = *(const uint4*)(in + (size_t)(tr * 64 + r) * C + tc * 64 + c8 * 8);
    u32* t = (u32*)(tile + r * 66 + c8 * 8);
    t[0] = raw.x; t[1] = raw.y; t[2] = raw.z; t[3] = raw.w;
  }
  __syncthreads();
#pragma unroll
  for (int i = 0; i < 2; ++i) {
    int v = tid + i * 256;
    int c = v >> 3, r8 = v & 7;
    u32 w[4];
#pragma unroll
    for (int j = 0; j < 4; ++j)
      w[j] = (u32)tile[(r8 * 8 + 2 * j) * 66 + c] | ((u32)tile[(r8 * 8 + 2 * j + 1) * 66 + c] << 16);
    *(uint4*)(out + (size_t)(tc * 64 + c) * R + tr * 64 + r8 * 8) = make_uint4(w[0], w[1], w[2], w[3]);
  }
}

__global__ __launch_bounds__(256) void k1_prep(const u16* __restrict__ x, const u16* __restrict__ logA,
                                               const u16* __restrict__ Aim, const u16* __restrict__ Cre,
                                               const u16* __restrict__ Cim, const u16* __restrict__ logdt,
                                               const u16* __restrict__ w1, const u16* __restrict__ w2, char* ws) {
  __shared__ __attribute__((aligned(16))) char smem[81920];
  const int b = blockIdx.x;
  if (b < DD) {
    kgen_role(b, logA, Aim, Cre, Cim, logdt, (u16*)(ws + WS_KP), smem);
  } else if (b < DD + 64) {
    ln_role(b - DD, x, (u16*)(ws + WS_XN), (u16*)(ws + WS_XNT), smem);
  } else {
    int t = b - DD - 64;
    tr_role(w1, (u16*)(ws + WS_W1T), DD, FF, t / 28, t % 28, smem);
    tr_role(w2, (u16*)(ws + WS_W2T), FF, DD, t / 7, t % 7, smem);
  }
}

__device__ void gemm1_role(int blk, const u16* __restrict__ xn, const u16* __restrict__ w1T,
                           const u16* __restrict__ b1, u16* __restrict__ h, char* smem) {
  const int tid = threadIdx.x, wave = tid >> 6, lane = tid & 63;
  const int xcd = blk & 7, idx = blk >> 3;
  const int mt = xcd * 2 + (idx & 1), nt = idx >> 1;
  const int l0 = mt * 128, f0 = nt * 128;
  const int wr = wave >> 1, wc = wave & 1;
  const int r32 = lane & 31, hh = lane >> 5;

  auto stage = [&](int t, int buf) {
    char* bA = smem + buf * 32768;
    char* bB = bA + 16384;
#pragma unroll
    for (int i = 0; i < 4; ++i) {
      int piece = wave * 4 + i;
      int row = piece * 8 + (lane >> 3);
      int src = (lane & 7) ^ ((row >> 1) & 7);
      __builtin_amdgcn_global_load_lds((const u32*)(w1T + (size_t)(f0 + row) * DD + t * 64 + src * 8),
                                       (u32*)(bA + piece * 1024 + lane * 16), 16, 0, 0);
      __builtin_amdgcn_global_load_lds((const u32*)(xn + (size_t)(l0 + row) * DD + t * 64 + src * 8),
                                       (u32*)(bB + piece * 1024 + lane * 16), 16, 0, 0);
    }
  };
  f32x16 acc[2][2];
#pragma unroll
  for (int i = 0; i < 2; ++i)
#pragma unroll
    for (int j = 0; j < 2; ++j)
#pragma unroll
      for (int r = 0; r < 16; ++r) acc[i][j][r] = 0.f;

  stage(0, 0);
  asm volatile("s_waitcnt vmcnt(0)" ::: "memory");
  __syncthreads();
  for (int t = 0; t < 7; ++t) {
    int buf = t & 1;
    if (t + 1 < 7) stage(t + 1, buf ^ 1);
    const char* bA = smem + buf * 32768;
    const char* bB = bA + 16384;
#pragma unroll
    for (int kk = 0; kk < 4; ++kk) {
      bf16x8 a[2], bq[2];
#pragma unroll
      for (int i = 0; i < 2; ++i) {
        int row = wr * 64 + i * 32 + r32;
        a[i] = *(const bf16x8*)(bA + row * 128 + (((2 * kk + hh) ^ ((row >> 1) & 7)) << 4));
        int rowb = wc * 64 + i * 32 + r32;
        bq[i] = *(const bf16x8*)(bB + rowb * 128 + (((2 * kk + hh) ^ ((rowb >> 1) & 7)) << 4));
      }
#pragma unroll
      for (int i = 0; i < 2; ++i)
#pragma unroll
        for (int j = 0; j < 2; ++j)
          acc[i][j] = __builtin_amdgcn_mfma_f32_32x32x16_bf16(a[i], bq[j], acc[i][j], 0, 0, 0);
    }
    asm volatile("s_waitcnt vmcnt(0)" ::: "memory");
    __syncthreads();
  }
#pragma unroll
  for (int i = 0; i < 2; ++i)
#pragma unroll
    for (int g = 0; g < 4; ++g) {
      int f = f0 + wr * 64 + i * 32 + 8 * g + 4 * hh;
      uint2 braw = *(const uint2*)(b1 + f);
      float bb[4] = {lo16f(braw.x), hi16f(braw.x), lo16f(braw.y), hi16f(braw.y)};
#pragma unroll
      for (int j = 0; j < 2; ++j) {
        int l = l0 + wc * 64 + j * 32 + r32;
        float o[4];
#pragma unroll
        for (int e = 0; e < 4; ++e) {
          float v = rbf(rbf(acc[i][j][4 * g + e]) + bb[e]);
          v = v > 0.f ? v : 0.f;
          o[e] = v * v;
        }
        *(uint2*)(h + (size_t)l * FF + f) = make_uint2(pack2(o[0], o[1]), pack2(o[2], o[3]));
      }
    }
}

#define CV_KOFF 0
#define CV_WOFF 9216
#define CV_PHIP 5136
#define CV_ZOFF (CV_WOFF + 8 * CV_PHIP)
__device__ void conv_role(int d, const u16* __restrict__ xnT, const u16* __restrict__ Kp,
                          const u16* __restrict__ Dsk, u16* __restrict__ sT, char* smem) {
  const int tid = threadIdx.x, wave = tid >> 6, lane = tid & 63;
  {
    const uint4* src = (const uint4*)(Kp + (size_t)d * 2 * KPAD);
    uint4* dst = (uint4*)(smem + CV_KOFF);
#pragma unroll
    for (int i = 0; i < 3; ++i) {
      int v = tid + i * 256;
      if (v < 576) dst[v] = src[v];
    }
  }
  const uint4* usrc = (const uint4*)(xnT + (size_t)d * LL);
  uint4 cur = usrc[tid];
  uint4 prv = tid > 0 ? usrc[tid - 1] : make_uint4(0, 0, 0, 0);
  {
    u32 Dw[8] = {prv.x, prv.y, prv.z, prv.w, cur.x, cur.y, cur.z, cur.w};
    char* wb = smem + CV_WOFF + (tid >> 5) * 640 + (tid & 31) * 16;
#pragma unroll
    for (int phi = 0; phi < 8; ++phi) {
      int e = phi + 1;
      uint4 o;
      if ((e & 1) == 0) {
        int k = e >> 1;
        o = make_uint4(Dw[k], Dw[k + 1], Dw[k + 2], Dw[k + 3]);
      } else {
        int k = e >> 1;
        o.x = __builtin_amdgcn_alignbit(Dw[k + 1], Dw[k], 16);
        o.y = __builtin_amdgcn_alignbit(Dw[k + 2], Dw[k + 1], 16);
        o.z = __builtin_amdgcn_alignbit(Dw[k + 3], Dw[k + 2], 16);
        o.w = __builtin_amdgcn_alignbit(Dw[k + 4], Dw[k + 3], 16);
      }
      *(uint4*)(wb + phi * CV_PHIP) = o;
    }
    if (tid == 0) *(uint4*)(smem + CV_ZOFF) = make_uint4(0, 0, 0, 0);
  }
  __syncthreads();

  const int r32 = lane & 31, hh = lane >> 5;
  const int phi = r32 & 7, al = r32 >> 3;
  f32x16 acc0, acc1;
#pragma unroll
  for (int r = 0; r < 16; ++r) { acc0[r] = 0.f; acc1[r] = 0.f; }
  for (int dl = 0; dl < 4; ++dl) {
#pragma unroll
    for (int tt = 0; tt < 4; ++tt) {
      int t = wave * 4 + tt;
      int v = 2 * t + hh;
      int aoff = CV_KOFF + 512 + 512 * dl + 16 * (r32 - v);
      bf16x8 a0h = *(const bf16x8*)(smem + aoff);
      bf16x8 a0l = *(const bf16x8*)(smem + aoff + 4608);
      bf16x8 a1h = *(const bf16x8*)(smem + aoff + 2048);
      bf16x8 a1l = *(const bf16x8*)(smem + aoff + 2048 + 4608);
      int ciX = al - dl;
      int xoff = ciX < 0 ? CV_ZOFF : (CV_WOFF + phi * CV_PHIP + ciX * 640 + v * 16);
      int zoff = CV_WOFF + phi * CV_PHIP + (ciX + 4) * 640 + v * 16;
      bf16x8 X = *(const bf16x8*)(smem + xoff);
      bf16x8 Z = *(const bf16x8*)(smem + zoff);
      acc0 = __builtin_amdgcn_mfma_f32_32x32x16_bf16(a0l, X, acc0, 0, 0, 0);
      acc0 = __builtin_amdgcn_mfma_f32_32x32x16_bf16(a0h, X, acc0, 0, 0, 0);
      acc1 = __builtin_amdgcn_mfma_f32_32x32x16_bf16(a1l, X, acc1, 0, 0, 0);
      acc1 = __builtin_amdgcn_mfma_f32_32x32x16_bf16(a1h, X, acc1, 0, 0, 0);
      acc1 = __builtin_amdgcn_mfma_f32_32x32x16_bf16(a0l, Z, acc1, 0, 0, 0);
      acc1 = __builtin_amdgcn_mfma_f32_32x32x16_bf16(a0h, Z, acc1, 0, 0, 0);
    }
  }
  __syncthreads();
  float* Y = (float*)(smem + CV_WOFF);
#pragma unroll
  for (int reg = 0; reg < 16; ++reg) {
    int r = (reg & 3) + 8 * (reg >> 2) + 4 * hh;
    int l = 8 * r + phi + 256 * al;
    Y[wave * 2048 + l] = acc0[reg];
    Y[wave * 2048 + l + 1024] = acc1[reg];
  }
  __syncthreads();
  {
    float y[8];
#pragma unroll
    for (int j = 0; j < 8; ++j) y[j] = 0.f;
#pragma unroll
    for (int w = 0; w < 4; ++w) {
      const float4* p = (const float4*)(Y + w * 2048 + 8 * tid);
      float4 a = p[0], b = p[1];
      y[0] += a.x; y[1] += a.y; y[2] += a.z; y[3] += a.w;
      y[4] += b.x; y[5] += b.y; y[6] += b.z; y[7] += b.w;
    }
    float dk = bf2f(Dsk[d]);
    u32 cw[4] = {cur.x, cur.y, cur.z, cur.w};
    u32 o[4];
#pragma unroll
    for (int j = 0; j < 4; ++j) {
      float s0 = rbf(y[2 * j]) + rbf(lo16f(cw[j]) * dk);
      float s1 = rbf(y[2 * j + 1]) + rbf(hi16f(cw[j]) * dk);
      o[j] = pack2(s0, s1);
    }
    *(uint4*)(sT + (size_t)d * LL + 8 * tid) = make_uint4(o[0], o[1], o[2], o[3]);
  }
}

__global__ __launch_bounds__(256) void k2_gemm1_conv(const u16* __restrict__ b1, const u16* __restrict__ Dsk, char* ws) {
  __shared__ __attribute__((aligned(16))) char smem[65536];
  const int b = blockIdx.x;
  if (b < 224) {
    gemm1_role(b, (const u16*)(ws + WS_XN), (const u16*)(ws + WS_W1T), b1, (u16*)(ws + WS_H), smem);
  } else {
    conv_role(b - 224, (const u16*)(ws + WS_XNT), (const u16*)(ws + WS_KP), Dsk, (u16*)(ws + WS_ST), smem);
  }
}

__global__ __launch_bounds__(256) void k3_gemm2(const u16* __restrict__ x, const u16* __restrict__ b2,
                                                const char* __restrict__ ws, u16* __restrict__ out) {
  __shared__ __attribute__((aligned(16))) char smem[65536];
  const u16* h = (const u16*)(ws + WS_H);
  const u16* w2T = (const u16*)(ws + WS_W2T);
  const u16* sT = (const u16*)(ws + WS_ST);
  const int tid = threadIdx.x, wave = tid >> 6, lane = tid & 63;
  const int blk = blockIdx.x;
  const int xcd = blk & 7, idx = blk >> 3;
  const int mt = xcd * 4 + (idx & 3), nt = idx >> 2;
  const int l0 = mt * 64, d0 = nt * 64;
  const int wr = wave >> 1, wc = wave & 1;
  const int r32 = lane & 31, hh = lane >> 5;

  auto stage = [&](int t, int buf) {
    char* bA = smem + buf * 32768;
    char* bB = bA + 16384;
#pragma unroll
    for (int i = 0; i < 4; ++i) {
      int piece = wave * 4 + i;
      int row = piece * 4 + (lane >> 4);
      int src = (lane & 15) ^ (row & 15);
      __builtin_amdgcn_global_load_lds((const u32*)(w2T + (size_t)(d0 + row) * FF + t * 128 + src * 8),
                                       (u32*)(bA + piece * 1024 + lane * 16), 16, 0, 0);
      __builtin_amdgcn_global_load_lds((const u32*)(h + (size_t)(l0 + row) * FF + t * 128 + src * 8),
                                       (u32*)(bB + piece * 1024 + lane * 16), 16, 0, 0);
    }
  };
  f32x16 acc;
#pragma unroll
  for (int r = 0; r < 16; ++r) acc[r] = 0.f;
  stage(0, 0);
  asm volatile("s_waitcnt vmcnt(0)" ::: "memory");
  __syncthreads();
  const int rowa = wr * 32 + r32, rowb = wc * 32 + r32;
  for (int t = 0; t < 14; ++t) {
    int buf = t & 1;
    if (t + 1 < 14) stage(t + 1, buf ^ 1);
    const char* bA = smem + buf * 32768;
    const char* bB = bA + 16384;
#pragma unroll
    for (int kk = 0; kk < 8; ++kk) {
      bf16x8 a = *(const bf16x8*)(bA + rowa * 256 + (((2 * kk + hh) ^ (rowa & 15)) << 4));
      bf16x8 bq = *(const bf16x8*)(bB + rowb * 256 + (((2 * kk + hh) ^ (rowb & 15)) << 4));
      acc = __builtin_amdgcn_mfma_f32_32x32x16_bf16(a, bq, acc, 0, 0, 0);
    }
    asm volatile("s_waitcnt vmcnt(0)" ::: "memory");
    __syncthreads();
  }
  const int l = l0 + wc * 32 + r32;
#pragma unroll
  for (int g = 0; g < 4; ++g) {
    int d = d0 + wr * 32 + 8 * g + 4 * hh;
    uint2 braw = *(const uint2*)(b2 + d);
    uint2 xraw = *(const uint2*)(x + (size_t)l * DD + d);
    float bb[4] = {lo16f(braw.x), hi16f(braw.x), lo16f(braw.y), hi16f(braw.y)};
    float xx[4] = {lo16f(xraw.x), hi16f(xraw.x), lo16f(xraw.y), hi16f(xraw.y)};
    float o[4];
#pragma unroll
    for (int e = 0; e < 4; ++e) {
      float v = rbf(rbf(acc[4 * g + e]) + bb[e]);
      float gg = rbf(v / (1.f + expf(-v)));
      float s = bf2f(sT[(size_t)(d + e) * LL + l]);
      o[e] = xx[e] + rbf(s * gg);
    }
    *(uint2*)(out + (size_t)l * DD + d) = make_uint2(pack2(o[0], o[1]), pack2(o[2], o[3]));
  }
}

extern "C" void kernel_launch(void* const* d_in, const int* in_sizes, int n_in,
                              void* d_out, int out_size, void* d_ws, size_t ws_size,
                              hipStream_t stream) {
  const u16* x = (const u16*)d_in[0];
  const u16* logA = (const u16*)d_in[1];
  const u16* Aim = (const u16*)d_in[2];
  const u16* Cre = (const u16*)d_in[3];
  const u16* Cim = (const u16*)d_in[4];
  const u16* logdt = (const u16*)d_in[5];
  const u16* Dsk = (const u16*)d_in[6];
  const u16* w1 = (const u16*)d_in[8];
  const u16* b1 = (const u16*)d_in[9];
  const u16* w2 = (const u16*)d_in[10];
  const u16* b2 = (const u16*)d_in[11];
  char* ws = (char*)d_ws;
  k1_prep<<<DD + 64 + 196, 256, 0, stream>>>(x, logA, Aim, Cre, Cim, logdt, w1, w2, ws);
  k2_gemm1_conv<<<224 + DD, 256, 0, stream>>>(b1, Dsk, ws);
  k3_gemm2<<<224, 256, 0, stream>>>(x, b2, ws, (u16*)d_out);
}
```

```cpp
#include <hip/hip_runtime.h>
#include <hip/hip_bf16.h>
#include <stdint.h>

typedef __attribute__((ext_vector_type(8))) short bf16x8;
typedef __attribute__((ext_vector_type(4))) float f32x4;
typedef __attribute__((ext_vector_type(16))) float f32x16;
typedef unsigned int u32;
typedef unsigned short u16;

#define LL 2048
#define DD 448
#define NN 256
#define FF 1792
#define KPAD 2304

#define WS_XN 0
#define WS_XNT (WS_XN + LL * DD * 2)
#define WS_W1T (WS_XNT + LL * DD * 2)
#define WS_W2T (WS_W1T + DD * FF * 2)
#define WS_KP (WS_W2T + DD * FF * 2)
#define WS_H (WS_KP + DD * 2 * KPAD * 2)
#define WS_ST (WS_H + LL * FF * 2)

#define STAMP(K, I)
__device__ __forceinline__ float bf2f(u16 v) { return __uint_as_float((u32)v << 16); }
__device__ __forceinline__ u16 f2bf(float f) { __bf16 b = (__bf16)f; return __builtin_bit_cast(u16, b); }
__device__ __forceinline__ float rbf(float f) { return (float)(__bf16)f; }
__device__ __forceinline__ float lo16f(u32 v) { return __uint_as_float(v << 16); }
__device__ __forceinline__ float hi16f(u32 v) { return __uint_as_float(v & 0xffff0000u); }
__device__ __forceinline__ u32 pack2(float a, float b) { return (u32)f2bf(a) | ((u32)f2bf(b) << 16); }

__device__ __forceinline__ float wave_sum(float v) {
#pragma unroll
  for (int o = 32; o > 0; o >>= 1) v += __shfl_xor(v, o);
  return v;
}

typedef float f2 __attribute__((ext_vector_type(2)));
typedef __bf16 bf2 __attribute__((ext_vector_type(2)));
__device__ __forceinline__ f2 cmul(f2 a, f2 b) {
  f2 t = a.xx * b;
  f2 bs = {-b.y, b.x};
  return __builtin_elementwise_fma(a.yy, bs, t);
}
__device__ __forceinline__ f2 cexp_(float are, float aim) {
  float mag = __builtin_amdgcn_exp2f(are * 1.44269504088896340736f);
  const float I2P_HI = 0.15915493667125702f;
  const float I2P_LO = 6.4206383e-09f;
  float t = aim * I2P_HI;
  float e = fmaf(aim, I2P_HI, -t) + aim * I2P_LO;
  float f = (t - rintf(t)) + e;
  f2 r;
  r.x = mag * __builtin_amdgcn_cosf(f);
  r.y = mag * __builtin_amdgcn_sinf(f);
  return r;
}

__device__ __forceinline__ void kg_store(char* tabn, const int* sw, int idx, f2 v) {
  bf2 h = __builtin_convertvector(v, bf2);
  f2 hf = __builtin_convertvector(h, f2);
  bf2 l = __builtin_convertvector(v - hf, bf2);
  uint2 w = make_uint2(__builtin_bit_cast(u32, h), __builtin_bit_cast(u32, l));
  *(uint2*)(tabn + idx * 1024 + sw[idx & 15]) = w;
}

__device__ void kgen_role(int d, const u16* __restrict__ logA, const u16* __restrict__ Aim,
                          const u16* __restrict__ Cre, const u16* __restrict__ Cim,
                          const u16* __restrict__ logdt, u16* __restrict__ Kp, char* smem) {
  const int tid = threadIdx.x, wave = tid >> 6, lane = tid & 63;
  const int role = __builtin_amdgcn_readfirstlane(tid >> 7);
  char* tabE = smem;
  char* tabG = smem + 40 * 1024;
  char* tab = role ? tabG : tabE;
  const int nl = tid & 127;
  u16 in_la[2], in_ai[2], in_cr[2], in_ci[2];
  const u16 in_dt = logdt[d];
#pragma unroll
  for (int pass = 0; pass < 2; ++pass) {
    in_la[pass] = logA[d * NN + pass * 128 + nl];
    in_ai[pass] = Aim[d * NN + pass * 128 + nl];
    in_cr[pass] = Cre[d * NN + pass * 128 + nl];
    in_ci[pass] = Cim[d * NN + pass * 128 + nl];
  }
  STAMP(0, 0);
  const float dt = rbf(expf(bf2f(in_dt)));
  f32x4 acc = {0.f, 0.f, 0.f, 0.f};
  float last = 0.f;

  const int seg = wave;
  const int fi = lane & 15, fq = lane >> 4;
  int gidx, eidx;
  if (seg == 0) { gidx = fi; eidx = fi; }
  else {
    gidx = 8 + 8 * seg + (fi & 7);
    int e = fi << seg;
    eidx = e < 16 ? e : (e < 32 ? 16 + ((e - 16) >> 1) : (e < 64 ? 24 + ((e - 32) >> 2) : 32 + ((e - 64) >> 3)));
  }
  int sw[16];
#pragma unroll
  for (int i = 0; i < 16; ++i) sw[i] = (((nl >> 1) ^ i) << 4) + ((nl & 1) << 3);

#pragma unroll
  for (int pass = 0; pass < 2; ++pass) {
    {
      float ea = rbf(expf(bf2f(in_la[pass])));
      float sc = role ? 16.f : 1.f;
      float are = (-ea * dt) * sc;
      float aim = (bf2f(in_ai[pass]) * dt) * sc;
      if (role == 0) {
#define PUT(IDX, V) kg_store(tab, sw, IDX, V);
        {
          f2 b0 = {1.f, 0.f};
          f2 b1 = cexp_(are, aim), b2 = cexp_(are * 2.f, aim * 2.f), b4 = cexp_(are * 4.f, aim * 4.f);
          f2 b8 = cexp_(are * 8.f, aim * 8.f), b16 = cexp_(are * 16.f, aim * 16.f);
          f2 b32 = cexp_(are * 32.f, aim * 32.f), b64 = cexp_(are * 64.f, aim * 64.f);
          f2 b3 = cmul(b2, b1), b5 = cmul(b4, b1), b6 = cmul(b4, b2), b7 = cmul(b4, b3);
          f2 b9 = cmul(b8, b1), b10 = cmul(b8, b2), b11 = cmul(b8, b3), b12 = cmul(b8, b4);
          f2 b13 = cmul(b8, b5), b14 = cmul(b8, b6), b15 = cmul(b8, b7);
          PUT(0, b0) PUT(1, b1) PUT(2, b2) PUT(3, b3) PUT(4, b4) PUT(5, b5) PUT(6, b6) PUT(7, b7)
          PUT(8, b8) PUT(9, b9) PUT(10, b10) PUT(11, b11) PUT(12, b12) PUT(13, b13) PUT(14, b14) PUT(15, b15)
          f2 b18 = cmul(b16, b2), b20 = cmul(b16, b4), b22 = cmul(b16, b6), b24 = cmul(b16, b8);
          f2 b26 = cmul(b16, b10), b28 = cmul(b16, b12), b30 = cmul(b16, b14);
          PUT(16, b16) PUT(17, b18) PUT(18, b20) PUT(19, b22) PUT(20, b24) PUT(21, b26) PUT(22, b28) PUT(23, b30)
          f2 b36 = cmul(b32, b4), b40 = cmul(b32, b8), b44 = cmul(b32, b12), b48 = cmul(b32, b16);
          f2 b52 = cmul(b32, b20), b56 = cmul(b32, b24), b60 = cmul(b32, b28);
          PUT(24, b32) PUT(25, b36) PUT(26, b40) PUT(27, b44) PUT(28, b48) PUT(29, b52) PUT(30, b56) PUT(31, b60)
          f2 b72 = cmul(b64, b8), b80 = cmul(b64, b16), b88 = cmul(b64, b24), b96 = cmul(b64, b32);
          f2 b104 = cmul(b64, b40), b112 = cmul(b64, b48), b120 = cmul(b64, b56);
          PUT(32, b64) PUT(33, b72) PUT(34, b80) PUT(35, b88) PUT(36, b96) PUT(37, b104) PUT(38, b112) PUT(39, b120)
        }
#undef PUT
      } else {
        f2 c;
        c.x = bf2f(in_cr[pass]) * dt;
        c.y = bf2f(in_ci[pass]) * dt;
#define PUT(IDX, V) { f2 _t = cmul(c, V); _t.y = -_t.y; kg_store(tab, sw, IDX, _t); }
        {
          f2 b0 = {1.f, 0.f};
          f2 b1 = cexp_(are, aim), b2 = cexp_(are * 2.f, aim * 2.f), b4 = cexp_(are * 4.f, aim * 4.f);
          f2 b8 = cexp_(are * 8.f, aim * 8.f), b16 = cexp_(are * 16.f, aim * 16.f);
          f2 b32 = cexp_(are * 32.f, aim * 32.f), b64 = cexp_(are * 64.f, aim * 64.f);
          f2 b3 = cmul(b2, b1), b5 = cmul(b4, b1), b6 = cmul(b4, b2), b7 = cmul(b4, b3);
          f2 b9 = cmul(b8, b1), b10 = cmul(b8, b2), b11 = cmul(b8, b3), b12 = cmul(b8, b4);
          f2 b13 = cmul(b8, b5), b14 = cmul(b8, b6), b15 = cmul(b8, b7);
          PUT(0, b0) PUT(1, b1) PUT(2, b2) PUT(3, b3) PUT(4, b4) PUT(5, b5) PUT(6, b6) PUT(7, b7)
          PUT(8, b8) PUT(9, b9) PUT(10, b10) PUT(11, b11) PUT(12, b12) PUT(13, b13) PUT(14, b14) PUT(15, b15)
          f2 b18 = cmul(b16, b2), b20 = cmul(b16, b4), b22 = cmul(b16, b6), b24 = cmul(b16, b8);
          f2 b26 = cmul(b16, b10), b28 = cmul(b16, b12), b30 = cmul(b16, b14);
          PUT(16, b16) PUT(17, b18) PUT(18, b20) PUT(19, b22) PUT(20, b24) PUT(21, b26) PUT(22, b28) PUT(23, b30)
          f2 b36 = cmul(b32, b4), b40 = cmul(b32, b8), b44 = cmul(b32, b12), b48 = cmul(b32, b16);
          f2 b52 = cmul(b32, b20), b56 = cmul(b32, b24), b60 = cmul(b32, b28);
          PUT(24, b32) PUT(25, b36) PUT(26, b40) PUT(27, b44) PUT(28, b48) PUT(29, b52) PUT(30, b56) PUT(31, b60)
          f2 b72 = cmul(b64, b8), b80 = cmul(b64, b16), b88 = cmul(b64, b24), b96 = cmul(b64, b32);
          f2 b104 = cmul(b64, b40), b112 = cmul(b64, b48), b120 = cmul(b64, b56);
          PUT(32, b64) PUT(33, b72) PUT(34, b80) PUT(35, b88) PUT(36, b96) PUT(37, b104) PUT(38, b112) PUT(39, b120)
        }
#undef PUT
        f2 b128 = cexp_(are * 128.f, aim * 128.f);
        last += cmul(c, b128).x;
      }
    }
    STAMP(0, 1 + pass * 3);
    __syncthreads();
    STAMP(0, 2 + pass * 3);
#pragma unroll
    for (int kb = 0; kb < 2; ++kb) {
      uint4 g[8], e[8];
#pragma unroll
      for (int ks = 0; ks < 8; ++ks) {
        int chunk = (kb * 8 + ks) * 4 + fq;
        g[ks] = *(const uint4*)(tabG + gidx * 1024 + ((chunk ^ (gidx & 15)) << 4));
        e[ks] = *(const uint4*)(tabE + eidx * 1024 + ((chunk ^ (eidx & 15)) << 4));
      }
      __builtin_amdgcn_sched_barrier(0);
#pragma unroll
      for (int ks = 0; ks < 8; ++ks) {
        uint4 es = make_uint4(e[ks].y, e[ks].x, e[ks].w, e[ks].z);
        acc = __builtin_amdgcn_mfma_f32_16x16x32_bf16(__builtin_bit_cast(bf16x8, g[ks]), __builtin_bit_cast(bf16x8, es), acc, 0, 0, 0);
        acc = __builtin_amdgcn_mfma_f32_16x16x32_bf16(__builtin_bit_cast(bf16x8, g[ks]), __builtin_bit_cast(bf16x8, e[ks]), acc, 0, 0, 0);
      }
      __builtin_amdgcn_sched_barrier(0);
    }
    __syncthreads();
    STAMP(0, 3 + pass * 3);
  }
  float* Kd = (float*)smem;
  float* red = Kd + 1024;
  last = wave_sum(last);
  if (lane == 0) red[wave] = last;
#pragma unroll
  for (int r = 0; r < 4; ++r) {
    int row = fq * 4 + r;
    if (seg == 0) Kd[row * 16 + fi] = 2.f * acc[r];
    else if (row < 8) Kd[256 + (seg - 1) * 128 + row * 16 + fi] = 2.f * acc[r];
  }
  __syncthreads();
  if (tid == 0) Kd[640] = 2.f * (red[2] + red[3]);
  __syncthreads();
  u16* outh = Kp + (size_t)d * 2 * KPAD;
  u16* outl = outh + KPAD;
  {
    u32 wh[4], wl[4];
#pragma unroll
    for (int jj = 0; jj < 4; ++jj) {
      float kv[2];
#pragma unroll
      for (int e = 0; e < 2; ++e) {
        int l = 8 * tid + 7 - (2 * jj + e);
        int p = (int)rbf((float)l);
        int idx;
        if (p < 256) idx = p;
        else {
          int s = (31 - __clz(p)) - 7;
          idx = 256 + (s - 1) * 128 + ((p >> s) - 128);
        }
        kv[e] = Kd[idx];
      }
      u16 h0 = f2bf(kv[0]), h1 = f2bf(kv[1]);
      wh[jj] = (u32)h0 | ((u32)h1 << 16);
      wl[jj] = pack2(kv[0] - bf2f(h0), kv[1] - bf2f(h1));
    }
    *(uint4*)(outh + 256 + 8 * tid) = make_uint4(wh[0], wh[1], wh[2], wh[3]);
    *(uint4*)(outl + 256 + 8 * tid) = make_uint4(wl[0], wl[1], wl[2], wl[3]);
    if (tid < 32) {
      *(uint4*)(outh + 8 * tid) = make_uint4(0, 0, 0, 0);
      *(uint4*)(outl + 8 * tid) = make_uint4(0, 0, 0, 0);
    }
  }
  STAMP(0, 7);
}

__device__ void ln_role(int blk, const u16* __restrict__ x, u16* __restrict__ xn, u16* __restrict__ xnT, char* smem) {
  const int tid = threadIdx.x, wave = tid >> 6, lane = tid & 63;
  u16* tile = (u16*)smem;
  const int row = wave * 8 + (lane >> 3), c = lane & 7;
  const int l = blk * 32 + row;
  uint4 raw[7];
#pragma unroll
  for (int j = 0; j < 7; ++j) raw[j] = *(const uint4*)(x + (size_t)l * DD + j * 64 + c * 8);
  float s = 0.f;
#pragma unroll
  for (int j = 0; j < 7; ++j) {
    s += (lo16f(raw[j].x) + hi16f(raw[j].x)) + (lo16f(raw[j].y) + hi16f(raw[j].y));
    s += (lo16f(raw[j].z) + hi16f(raw[j].z)) + (lo16f(raw[j].w) + hi16f(raw[j].w));
  }
  s += __shfl_xor(s, 1); s += __shfl_xor(s, 2); s += __shfl_xor(s, 4);
  const float mean = s * (1.f / DD);
  float q = 0.f;
#pragma unroll
  for (int j = 0; j < 7; ++j) {
    u32 w4[4] = {raw[j].x, raw[j].y, raw[j].z, raw[j].w};
#pragma unroll
    for (int e = 0; e < 4; ++e) {
      float t0 = lo16f(w4[e]) - mean, t1 = hi16f(w4[e]) - mean;
      q = fmaf(t0, t0, q); q = fmaf(t1, t1, q);
    }
  }
  q += __shfl_xor(q, 1); q += __shfl_xor(q, 2); q += __shfl_xor(q, 4);
  const float var = q * (1.f / DD);
  const float mb = rbf(mean), vb = rbf(var);
  const float den = rbf(sqrtf(rbf(vb + 1e-5f)));
#pragma unroll
  for (int j = 0; j < 7; ++j) {
    u32 w4[4] = {raw[j].x, raw[j].y, raw[j].z, raw[j].w};
    u32 o[4];
#pragma unroll
    for (int e = 0; e < 4; ++e)
      o[e] = pack2(rbf(lo16f(w4[e]) - mb) / den, rbf(hi16f(w4[e]) - mb) / den);
    *(uint4*)(xn + (size_t)l * DD + j * 64 + c * 8) = make_uint4(o[0], o[1], o[2], o[3]);
    u32* trow = (u32*)(tile + row * 450 + j * 64 + c * 8);
    trow[0] = o[0]; trow[1] = o[1]; trow[2] = o[2]; trow[3] = o[3];
  }
  __syncthreads();
  for (int d = tid; d < DD; d += 256) {
    u32 w[16];
#pragma unroll
    for (int r = 0; r < 16; ++r)
      w[r] = (u32)tile[(2 * r) * 450 + d] | ((u32)tile[(2 * r + 1) * 450 + d] << 16);
    uint4* dst = (uint4*)(xnT + (size_t)d * LL + blk * 32);
    dst[0] = make_uint4(w[0], w[1], w[2], w[3]);
    dst[1] = make_uint4(w[4], w[5], w[6], w[7]);
    dst[2] = make_uint4(w[8], w[9], w[10], w[11]);
    dst[3] = make_uint4(w[12], w[13], w[14], w[15]);
  }
}

__device__ void tr_role(const u16* __restrict__ in, u16* __restrict__ out, int R, int C, int tr, int tc, char* smem) {
  const int tid = threadIdx.x;
  u16* tile = (u16*)smem;
  __syncthreads();
#pragma unroll
  for (int i = 0; i < 2; ++i) {
    int v = tid + i * 256;
    int r = v >> 3, c8 = v & 7;
    uint4 raw = *(const uint4*)(in + (size_t)(tr * 64 + r) * C + tc * 64 + c8 * 8);
    u32* t = (u32*)(tile + r * 66 + c8 * 8);
    t[0] = raw.x; t[1] = raw.y; t[2] = raw.z; t[3] = raw.w;
  }
  __syncthreads();
#pragma unroll
  for (int i = 0; i < 2; ++i) {
    int v = tid + i * 256;
    int c = v >> 3, r8 = v & 7;
    u32 w[4];
#pragma unroll
    for (int j = 0; j < 4; ++j)
      w[j] = (u32)tile[(r8 * 8 + 2 * j) * 66 + c] | ((u32)tile[(r8 * 8 + 2 * j + 1) * 66 + c] << 16);
    *(uint4*)(out + (size_t)(tc * 64 + c) * R + tr * 64 + r8 * 8) = make_uint4(w[0], w[1], w[2], w[3]);
  }
}

__global__ __launch_bounds__(256) void k1_prep(const u16* __restrict__ x, const u16* __restrict__ logA,
                                               const u16* __restrict__ Aim, const u16* __restrict__ Cre,
                                               const u16* __restrict__ Cim, const u16* __restrict__ logdt,
                                               const u16* __restrict__ w1, const u16* __restrict__ w2, char* ws, int boff) {
  __shared__ __attribute__((aligned(16))) char smem[81920];
  const int b = blockIdx.x + boff;
  if (b < DD) {
    kgen_role(b, logA, Aim, Cre, Cim, logdt, (u16*)(ws + WS_KP), smem);
  } else if (b < DD + 64) {
    ln_role(b - DD, x, (u16*)(ws + WS_XN), (u16*)(ws + WS_XNT), smem);
  } else {
    int t = b - DD - 64;
    tr_role(w1, (u16*)(ws + WS_W1T), DD, FF, t / 28, t % 28, smem);
    tr_role(w2, (u16*)(ws + WS_W2T), FF, DD, t / 7, t % 7, smem);
  }
}

#define SB() __builtin_amdgcn_sched_barrier(0)
__device__ void gemm1_role(int blk, const u16* __restrict__ xn, const u16* __restrict__ w1T,
                           const u16* __restrict__ b1, u16* __restrict__ h, char* smem) {
  const int tid = threadIdx.x, wave = tid >> 6, lane = tid & 63;
  const int xcd = blk & 7, idx = blk >> 3;
  const int mt = xcd * 2 + (idx & 1), nt = idx >> 1;
  const int l0 = mt * 128, f0 = nt * 128;
  const int wr = wave >> 1, wc = wave & 1;
  const int r32 = lane & 31, hh = lane >> 5;

  const u16* srcA[4];
  const u16* srcB[4];
#pragma unroll
  for (int i = 0; i < 4; ++i) {
    int row = (wave * 4 + i) * 8 + (lane >> 3);
    int src = (lane & 7) ^ ((row >> 1) & 7);
    srcA[i] = w1T + (size_t)(f0 + row) * DD + src * 8;
    srcB[i] = xn + (size_t)(l0 + row) * DD + src * 8;
  }
  auto gl = [&](int t, int q) {
    int i = q >> 1;
    char* dst = smem + (t % 3) * 32768 + (q & 1) * 16384 + (wave * 4 + i) * 1024 + lane * 16;
    const u16* sp = ((q & 1) ? srcB[i] : srcA[i]) + t * 64;
    __builtin_amdgcn_global_load_lds((const u32*)sp, (u32*)dst, 16, 0, 0);
  };
  int fo[4], fs[4];
#pragma unroll
  for (int w = 0; w < 4; ++w) {
    int row = ((w & 1) ? wc : wr) * 64 + (w >> 1) * 32 + r32;
    fo[w] = (w & 1) * 16384 + row * 128;
    fs[w] = (row >> 1) & 7;
  }
  f32x16 acc[2][2];
#pragma unroll
  for (int i = 0; i < 2; ++i)
#pragma unroll
    for (int j = 0; j < 2; ++j)
#pragma unroll
      for (int r = 0; r < 16; ++r) acc[i][j][r] = 0.f;

  STAMP(1, 0);
  uint2 braw_[2][4];
#pragma unroll
  for (int i = 0; i < 2; ++i)
#pragma unroll
    for (int g = 0; g < 4; ++g) braw_[i][g] = *(const uint2*)(b1 + f0 + wr * 64 + i * 32 + 8 * g + 4 * hh);
#pragma unroll
  for (int q = 0; q < 8; ++q) gl(0, q);
#pragma unroll
  for (int q = 0; q < 8; ++q) gl(1, q);
#pragma unroll
  for (int t = 0; t < 7; ++t) {
    if (t + 1 < 7) asm volatile("s_waitcnt vmcnt(8) lgkmcnt(0)" ::: "memory");
    else asm volatile("s_waitcnt vmcnt(0) lgkmcnt(0)" ::: "memory");
    __builtin_amdgcn_s_barrier();
    STAMP(1, 1 + t);
    const char* bs = smem + (t % 3) * 32768;
    bf16x8 fr[4][4];
#pragma unroll
    for (int w = 0; w < 4; ++w) fr[0][w] = *(const bf16x8*)(bs + fo[w] + ((hh ^ fs[w]) << 4));
    SB();
#pragma unroll
    for (int kk = 0; kk < 4; ++kk) {
#pragma unroll
      for (int m = 0; m < 4; ++m) {
        int i = m >> 1, j = m & 1;
        acc[i][j] = __builtin_amdgcn_mfma_f32_32x32x16_bf16(fr[kk][2 * i], fr[kk][2 * j + 1], acc[i][j], 0, 0, 0);
        if (kk < 3) fr[kk + 1][m] = *(const bf16x8*)(bs + fo[m] + (((2 * (kk + 1) + hh) ^ fs[m]) << 4));
        if ((m & 1) == 0 && t + 2 < 7) gl(t + 2, kk * 2 + (m >> 1));
        SB();
      }
    }
  }
  STAMP(1, 8);
  __syncthreads();
#pragma unroll
  for (int i = 0; i < 2; ++i)
#pragma unroll
    for (int g = 0; g < 4; ++g) {
      int fl = wr * 64 + i * 32 + 8 * g + 4 * hh;
      uint2 braw = braw_[i][g];
      float bb[4] = {lo16f(braw.x), hi16f(braw.x), lo16f(braw.y), hi16f(braw.y)};
#pragma unroll
      for (int j = 0; j < 2; ++j) {
        int ll = wc * 64 + j * 32 + r32;
        float o[4];
#pragma unroll
        for (int e = 0; e < 4; ++e) {
          float v = rbf(rbf(acc[i][j][4 * g + e]) + bb[e]);
          v = v > 0.f ? v : 0.f;
          o[e] = v * v;
        }
        *(uint2*)(smem + ll * 256 + (((fl >> 3) ^ (ll & 15)) << 4) + (fl & 4) * 2) =
            make_uint2(pack2(o[0], o[1]), pack2(o[2], o[3]));
      }
    }
  __syncthreads();
#pragma unroll
  for (int i = 0; i < 8; ++i) {
    int row = i * 16 + (tid >> 4), ch = tid & 15;
    uint4 v = *(const uint4*)(smem + row * 256 + ((ch ^ (row & 15)) << 4));
    *(uint4*)(h + (size_t)(l0 + row) * FF + f0 + ch * 8) = v;
  }
  STAMP(1, 9);
}

#define CV_KOFF 0
#define CV_WOFF 9216
#define CV_PHIP 5136
#define CV_ZOFF (CV_WOFF + 8 * CV_PHIP)
__device__ void conv_role(int d, const u16* __restrict__ xnT, const u16* __restrict__ Kp,
                          const u16* __restrict__ Dsk, u16* __restrict__ sT, char* smem) {
  const int tid = threadIdx.x, wave = tid >> 6, lane = tid & 63;
  STAMP(1, 0);
  const uint4* ksrc = (const uint4*)(Kp + (size_t)d * 2 * KPAD);
  const uint4* usrc = (const uint4*)(xnT + (size_t)d * LL);
  uint4 kv0 = ksrc[tid], kv1 = ksrc[tid + 256], kv2 = ksrc[tid < 64 ? tid + 512 : tid];
  uint4 cur = usrc[tid];
  uint4 prv = usrc[tid > 0 ? tid - 1 : 0];
  const u16 dkraw = Dsk[d];
  if (tid == 0) prv = make_uint4(0, 0, 0, 0);
  {
    uint4* dst = (uint4*)(smem + CV_KOFF);
    dst[tid] = kv0;
    dst[tid + 256] = kv1;
    if (tid < 64) dst[tid + 512] = kv2;
  }
  {
    u32 Dw[8] = {prv.x, prv.y, prv.z, prv.w, cur.x, cur.y, cur.z, cur.w};
    char* wb = smem + CV_WOFF + (tid >> 5) * 640 + (tid & 31) * 16;
#pragma unroll
    for (int phi = 0; phi < 8; ++phi) {
      int e = phi + 1;
      uint4 o;
      if ((e & 1) == 0) {
        int k = e >> 1;
        o = make_uint4(Dw[k], Dw[k + 1], Dw[k + 2], Dw[k + 3]);
      } else {
        int k = e >> 1;
        o.x = __builtin_amdgcn_alignbit(Dw[k + 1], Dw[k], 16);
        o.y = __builtin_amdgcn_alignbit(Dw[k + 2], Dw[k + 1], 16);
        o.z = __builtin_amdgcn_alignbit(Dw[k + 3], Dw[k + 2], 16);
        o.w = __builtin_amdgcn_alignbit(Dw[k + 4], Dw[k + 3], 16);
      }
      *(uint4*)(wb + phi * CV_PHIP) = o;
    }
    if (tid == 0) *(uint4*)(smem + CV_ZOFF) = make_uint4(0, 0, 0, 0);
  }
  STAMP(1, 1);
  __syncthreads();
  STAMP(1, 2);

  const int r32 = lane & 31, hh = lane >> 5;
  const int phi = r32 & 7, al = r32 >> 3;
  f32x16 acc0, acc1;
#pragma unroll
  for (int r = 0; r < 16; ++r) { acc0[r] = 0.f; acc1[r] = 0.f; }
  {
    bf16x8 fr[2][6];
    auto rd = [&](int g, int w) -> bf16x8 {
      int dl = g >> 2, tt = g & 3;
      int v = 2 * (wave * 4 + tt) + hh;
      int off;
      if (w < 4) {
        off = CV_KOFF + 512 + 512 * dl + 16 * (r32 - v) + (w & 1) * 4608 + (w >> 1) * 2048;
      } else {
        int ci = al - dl + (w - 4) * 4;
        off = ci < 0 ? CV_ZOFF : (CV_WOFF + phi * CV_PHIP + ci * 640 + v * 16);
      }
      return *(const bf16x8*)(smem + off);
    };
#pragma unroll
    for (int w = 0; w < 6; ++w) fr[0][w] = rd(0, w);
    SB();
#pragma unroll
    for (int g = 0; g < 16; ++g) {
      const int c = g & 1, n = c ^ 1;
      acc0 = __builtin_amdgcn_mfma_f32_32x32x16_bf16(fr[c][1], fr[c][4], acc0, 0, 0, 0);
      if (g < 15) fr[n][4] = rd(g + 1, 4);
      SB();
      acc1 = __builtin_amdgcn_mfma_f32_32x32x16_bf16(fr[c][3], fr[c][4], acc1, 0, 0, 0);
      if (g < 15) fr[n][1] = rd(g + 1, 1);
      SB();
      acc0 = __builtin_amdgcn_mfma_f32_32x32x16_bf16(fr[c][0], fr[c][4], acc0, 0, 0, 0);
      if (g < 15) fr[n][3] = rd(g + 1, 3);
      SB();
      acc1 = __builtin_amdgcn_mfma_f32_32x32x16_bf16(fr[c][2], fr[c][4], acc1, 0, 0, 0);
      if (g < 15) fr[n][0] = rd(g + 1, 0);
      SB();
      acc1 = __builtin_amdgcn_mfma_f32_32x32x16_bf16(fr[c][1], fr[c][5], acc1, 0, 0, 0);
      if (g < 15) fr[n][2] = rd(g + 1, 2);
      SB();
      acc1 = __builtin_amdgcn_mfma_f32_32x32x16_bf16(fr[c][0], fr[c][5], acc1, 0, 0, 0);
      if (g < 15) fr[n][5] = rd(g + 1, 5);
      SB();
    }
  }
  STAMP(1, 3);
  __syncthreads();
  float* Y = (float*)(smem + CV_WOFF);
#pragma unroll
  for (int reg = 0; reg < 16; ++reg) {
    int r = (reg & 3) + 8 * (reg >> 2) + 4 * hh;
    int l = 8 * r + phi + 256 * al;
    Y[wave * 2048 + l] = acc0[reg];
    Y[wave * 2048 + l + 1024] = acc1[reg];
  }
  __syncthreads();
  {
    float y[8];
#pragma unroll
    for (int j = 0; j < 8; ++j) y[j] = 0.f;
#pragma unroll
    for (int w = 0; w < 4; ++w) {
      const float4* p = (const float4*)(Y + w * 2048 + 8 * tid);
      float4 a = p[0], b = p[1];
      y[0] += a.x; y[1] += a.y; y[2] += a.z; y[3] += a.w;
      y[4] += b.x; y[5] += b.y; y[6] += b.z; y[7] += b.w;
    }
    float dk = bf2f(dkraw);
    u32 cw[4] = {cur.x, cur.y, cur.z, cur.w};
    u32 o[4];
#pragma unroll
    for (int j = 0; j < 4; ++j) {
      float s0 = rbf(y[2 * j]) + rbf(lo16f(cw[j]) * dk);
      float s1 = rbf(y[2 * j + 1]) + rbf(hi16f(cw[j]) * dk);
      o[j] = pack2(s0, s1);
    }
    *(uint4*)(sT + (size_t)d * LL + 8 * tid) = make_uint4(o[0], o[1], o[2], o[3]);
  }
  STAMP(1, 4);
}

__global__ __launch_bounds__(256) void k2_gemm1_conv(const u16* __restrict__ b1, const u16* __restrict__ Dsk, char* ws, int boff) {
  __shared__ __attribute__((aligned(16))) char smem[98304];
  const int b = blockIdx.x + boff;
  if (b < 224) {
    gemm1_role(b, (const u16*)(ws + WS_XN), (const u16*)(ws + WS_W1T), b1, (u16*)(ws + WS_H), smem);
  } else {
    conv_role(b - 224, (const u16*)(ws + WS_XNT), (const u16*)(ws + WS_KP), Dsk, (u16*)(ws + WS_ST), smem);
  }
}

__global__ __launch_bounds__(512) void k3_gemm2(const u16* __restrict__ x, const u16* __restrict__ b2,
                                                const char* __restrict__ ws, u16* __restrict__ out) {
  __shared__ __attribute__((aligned(16))) char smem[131072];
  const u16* h = (const u16*)(ws + WS_H);
  const u16* w2T = (const u16*)(ws + WS_W2T);
  const u16* sT = (const u16*)(ws + WS_ST);
  const int tid = threadIdx.x, wave = tid >> 6, lane = tid & 63;
  const int blk = blockIdx.x;
  const int xcd = blk & 7, idx = blk >> 3;
  const int mt = xcd * 4 + (idx & 3), nt = idx >> 2;
  const int l0 = mt * 64, d0 = nt * 64;
  const int r32 = lane & 31, hh = lane >> 5;

  const u16* srcA[2];
  const u16* srcB[2];
#pragma unroll
  for (int i = 0; i < 2; ++i) {
    int row = (wave * 2 + i) * 4 + (lane >> 4);
    int src = (lane & 15) ^ (row & 15);
    srcA[i] = w2T + (size_t)(d0 + row) * FF + src * 8;
    srcB[i] = h + (size_t)(l0 + row) * FF + src * 8;
  }
  auto gl = [&](int t, int q) {
    int i = q >> 1;
    char* dst = smem + (t & 3) * 32768 + (q & 1) * 16384 + (wave * 2 + i) * 1024 + lane * 16;
    const u16* sp = ((q & 1) ? srcB[i] : srcA[i]) + t * 128;
    __builtin_amdgcn_global_load_lds((const u32*)sp, (u32*)dst, 16, 0, 0);
  };
  f32x16 acc[2][2];
#pragma unroll
  for (int i = 0; i < 2; ++i)
#pragma unroll
    for (int j = 0; j < 2; ++j)
#pragma unroll
      for (int r = 0; r < 16; ++r) acc[i][j][r] = 0.f;
  STAMP(2, 0);
#pragma unroll
  for (int t = 0; t < 3; ++t)
#pragma unroll
    for (int q = 0; q < 4; ++q) gl(t, q);
  asm volatile("" ::: "memory");
  const int quad = wave >> 1, half = wave & 1;
  const int wr = quad >> 1, wc = quad & 1;
  const int l = l0 + wc * 32 + r32;
  uint2 braw_[2], xraw_[2];
  u16 sraw_[8];
#pragma unroll
  for (int g2 = 0; g2 < 2; ++g2) {
    int d = d0 + wr * 32 + 8 * (half * 2 + g2) + 4 * hh;
    braw_[g2] = *(const uint2*)(b2 + d);
    xraw_[g2] = *(const uint2*)(x + (size_t)l * DD + d);
#pragma unroll
    for (int e = 0; e < 4; ++e) sraw_[4 * g2 + e] = sT[(size_t)(d + e) * LL + l];
  }
  asm volatile("" ::: "memory");
  int fo[4];
#pragma unroll
  for (int w = 0; w < 4; ++w) {
    int row = (w >> 1) * 32 + r32;
    int ch = 2 * wave + hh;
    fo[w] = (w & 1) * 16384 + row * 256 + ((ch ^ (row & 15)) << 4);
  }
#pragma unroll
  for (int t = 0; t < 14; ++t) {
    if (t < 3) asm volatile("s_waitcnt vmcnt(20) lgkmcnt(0)" ::: "memory");
    else if (t + 2 < 14) asm volatile("s_waitcnt vmcnt(8) lgkmcnt(0)" ::: "memory");
    else if (t + 1 < 14) asm volatile("s_waitcnt vmcnt(4) lgkmcnt(0)" ::: "memory");
    else asm volatile("s_waitcnt vmcnt(0) lgkmcnt(0)" ::: "memory");
    __builtin_amdgcn_s_barrier();
    STAMP(2, 1 + t);
    const char* bs = smem + (t & 3) * 32768;
    bf16x8 fr[4];
#pragma unroll
    for (int w = 0; w < 4; ++w) fr[w] = *(const bf16x8*)(bs + fo[w]);
    SB();
#pragma unroll
    for (int m = 0; m < 4; ++m) {
      int i = m >> 1, j = m & 1;
      if (t + 3 < 14) gl(t + 3, m);
      acc[i][j] = __builtin_amdgcn_mfma_f32_32x32x16_bf16(fr[2 * i], fr[2 * j + 1], acc[i][j], 0, 0, 0);
      SB();
    }
  }
  __syncthreads();
  float* red = (float*)smem;
#pragma unroll
  for (int i = 0; i < 2; ++i)
#pragma unroll
    for (int j = 0; j < 2; ++j)
#pragma unroll
      for (int r4 = 0; r4 < 4; ++r4)
        *(float4*)(red + ((wave * 4 + i * 2 + j) * 4 + r4) * 256 + lane * 4) =
            make_float4(acc[i][j][4 * r4], acc[i][j][4 * r4 + 1], acc[i][j][4 * r4 + 2], acc[i][j][4 * r4 + 3]);
  __syncthreads();
  float fin[8];
#pragma unroll
  for (int r = 0; r < 8; ++r) fin[r] = 0.f;
#pragma unroll
  for (int w = 0; w < 8; ++w)
#pragma unroll
    for (int g2 = 0; g2 < 2; ++g2) {
      float4 v = *(const float4*)(red + ((w * 4 + quad) * 4 + half * 2 + g2) * 256 + lane * 4);
      fin[4 * g2] += v.x; fin[4 * g2 + 1] += v.y; fin[4 * g2 + 2] += v.z; fin[4 * g2 + 3] += v.w;
    }
#pragma unroll
  for (int g2 = 0; g2 < 2; ++g2) {
    int d = d0 + wr * 32 + 8 * (half * 2 + g2) + 4 * hh;
    uint2 braw = braw_[g2];
    uint2 xraw = xraw_[g2];
    float bb[4] = {lo16f(braw.x), hi16f(braw.x), lo16f(braw.y), hi16f(braw.y)};
    float xx[4] = {lo16f(xraw.x), hi16f(xraw.x), lo16f(xraw.y), hi16f(xraw.y)};
    float o[4];
#pragma unroll
    for (int e = 0; e < 4; ++e) {
      float v = rbf(rbf(fin[4 * g2 + e]) + bb[e]);
      float gg = rbf(v * __builtin_amdgcn_rcpf(1.f + __builtin_amdgcn_exp2f(v * -1.44269504088896340736f)));
      float s = bf2f(sraw_[4 * g2 + e]);
      o[e] = xx[e] + rbf(s * gg);
    }
    *(uint2*)(out + (size_t)l * DD + d) = make_uint2(pack2(o[0], o[1]), pack2(o[2], o[3]));
  }
  STAMP(2, 15);
}

extern "C" void kernel_launch(void* const* d_in, const int* in_sizes, int n_in,
                              void* d_out, int out_size, void* d_ws, size_t ws_size,
                              hipStream_t stream) {
  const u16* x = (const u16*)d_in[0];
  const u16* logA = (const u16*)d_in[1];
  const u16* Aim = (const u16*)d_in[2];
  const u16* Cre = (const u16*)d_in[3];
  const u16* Cim = (const u16*)d_in[4];
  const u16* logdt = (const u16*)d_in[5];
  const u16* Dsk = (const u16*)d_in[6];
  const u16* w1 = (const u16*)d_in[8];
  const u16* b1 = (const u16*)d_in[9];
  const u16* w2 = (const u16*)d_in[10];
  const u16* b2 = (const u16*)d_in[11];
  char* ws = (char*)d_ws;
  k1_prep<<<DD + 64 + 196, 256, 0, stream>>>(x, logA, Aim, Cre, Cim, logdt, w1, w2, ws, 0);
  k2_gemm1_conv<<<224 + DD, 256, 0, stream>>>(b1, Dsk, ws, 0);
  k3_gemm2<<<224, 512, 0, stream>>>(x, b2, ws, (u16*)d_out);
}
```

```cpp
#include <hip/hip_runtime.h>
#include <hip/hip_bf16.h>
#include <stdint.h>

typedef __attribute__((ext_vector_type(8))) short bf16x8;
typedef __attribute__((ext_vector_type(4))) float f32x4;
typedef __attribute__((ext_vector_type(16))) float f32x16;
typedef unsigned int u32;
typedef unsigned short u16;

#define LL 2048
#define DD 448
#define NN 256
#define FF 1792
#define KPAD 2304

#define WS_XN 0
#define WS_XNT (WS_XN + LL * DD * 2)
#define WS_W1T (WS_XNT + LL * DD * 2)
#define WS_W2T (WS_W1T + DD * FF * 2)
#define WS_KP (WS_W2T + DD * FF * 2)
#define WS_H (WS_KP + DD * 2 * KPAD * 2)
#define WS_ST (WS_H + LL * FF * 2)
#define WS_CNT (WS_ST + LL * DD * 2)
#define WS_END (WS_CNT + 32 * 128)

#define STAMP(K, I)
#define STAMPV(K, I, V)
__device__ __forceinline__ float bf2f(u16 v) { return __uint_as_float((u32)v << 16); }
__device__ __forceinline__ u16 f2bf(float f) { __bf16 b = (__bf16)f; return __builtin_bit_cast(u16, b); }
__device__ __forceinline__ float rbf(float f) { return (float)(__bf16)f; }
__device__ __forceinline__ float lo16f(u32 v) { return __uint_as_float(v << 16); }
__device__ __forceinline__ float hi16f(u32 v) { return __uint_as_float(v & 0xffff0000u); }
__device__ __forceinline__ u32 pack2(float a, float b) { return (u32)f2bf(a) | ((u32)f2bf(b) << 16); }

__device__ __forceinline__ float fdiv(float a, float b, float r) {
  float q = a * r;
  float e = fmaf(-q, b, a);
  return fmaf(e, r, q);
}
__device__ __forceinline__ float wave_sum(float v) {
#pragma unroll
  for (int o = 32; o > 0; o >>= 1) v += __shfl_xor(v, o);
  return v;
}

typedef float f2 __attribute__((ext_vector_type(2)));
typedef __bf16 bf2 __attribute__((ext_vector_type(2)));
__device__ __forceinline__ f2 cmul(f2 a, f2 b) {
  f2 t = a.xx * b;
  f2 bs = {-b.y, b.x};
  return __builtin_elementwise_fma(a.yy, bs, t);
}
__device__ __forceinline__ f2 rbf2(f2 v) { return __builtin_convertvector(__builtin_convertvector(v, bf2), f2); }
__device__ __forceinline__ u32 sqrelu2(f2 a, f2 b) {
  f2 v = rbf2(rbf2(a) + b);
  f2 z = {0.f, 0.f};
  v = __builtin_elementwise_max(v, z);
  v = v * v;
  return __builtin_bit_cast(u32, __builtin_convertvector(v, bf2));
}
__device__ __forceinline__ f2 cexp_(float are, float aim) {
  float mag = __builtin_amdgcn_exp2f(are * 1.44269504088896340736f);
  const float I2P_HI = 0.15915493667125702f;
  const float I2P_LO = 6.4206383e-09f;
  float t = aim * I2P_HI;
  float e = fmaf(aim, I2P_HI, -t) + aim * I2P_LO;
  float f = (t - rintf(t)) + e;
  f2 r;
  r.x = mag * __builtin_amdgcn_cosf(f);
  r.y = mag * __builtin_amdgcn_sinf(f);
  return r;
}

#define SB() __builtin_amdgcn_sched_barrier(0)
__device__ __forceinline__ constexpr int ekey(int r) {
  return (int)((r < 16 ? 0x3607495fa3d0afd9ull >> (4 * r) : (r < 32 ? 0x32c4fc79db2a54d0ull >> (4 * (r - 16)) : 0x00000000e72ca570ull >> (4 * (r - 32)))) & 15ull);
}
__device__ __forceinline__ constexpr int gkey(int r) {
  return (int)((r < 16 ? 0x581b76be135f9cd3ull >> (4 * r) : (r < 32 ? 0x52fb08d7941be27dull >> (4 * (r - 16)) : 0x00000000906fb42cull >> (4 * (r - 32)))) & 15ull);
}
__device__ __forceinline__ void kg_store(char* tabn, int swb, int idx, int key, f2 v) {
  bf2 h = __builtin_convertvector(v, bf2);
  f2 hf = __builtin_convertvector(h, f2);
  bf2 l = __builtin_convertvector(v - hf, bf2);
  uint2 w = make_uint2(__builtin_bit_cast(u32, h), __builtin_bit_cast(u32, l));
  *(uint2*)(tabn + idx * 1024 + (swb ^ (key << 4))) = w;
}

#define EK_LO0 0x3607495fa3d0afd9ull
#define EK_LO1 0x000054d0u
#define EK_HI0 0xa57032c4fc79db2aull
#define EK_HI1 0x0000e72cu
#define GK_LO0 0x581b76be135f9cd3ull
#define GK_LO1 0x0000e27du
#define GK_HI0 0xb42c52fb08d7941bull
#define GK_HI1 0x0000906fu
__device__ __forceinline__ void kgen_role(int d, const u16* __restrict__ logA, const u16* __restrict__ Aim,
                                          const u16* __restrict__ Cre, const u16* __restrict__ Cim,
                                          const u16* __restrict__ logdt, u16* __restrict__ Kp,
                                          const u16* __restrict__ w1, const u16* __restrict__ w2,
                                          u16* __restrict__ w1T, u16* __restrict__ w2T, char* smem) {
  const int tid = threadIdx.x;
  const bool has_tr = d < 392;
  const int tt_ = d < 196 ? d : d - 196;
  const u16* tr_in = d < 196 ? w1 : w2;
  u16* tr_out = d < 196 ? w1T : w2T;
  const int trR = d < 196 ? DD : FF, trC = d < 196 ? FF : DD;
  const int tr_r = d < 196 ? tt_ / 28 : tt_ / 7, tr_c = d < 196 ? tt_ % 28 : tt_ % 7;
  uint4 tr_raw = make_uint4(0, 0, 0, 0);
  if (has_tr) tr_raw = *(const uint4*)(tr_in + (size_t)(tr_r * 64 + (tid >> 3)) * trC + tr_c * 64 + (tid & 7) * 8);
  const int wave = tid >> 6, lane = tid & 63;
  const int role = __builtin_amdgcn_readfirstlane(tid >> 7);
  char* tabE = smem;
  char* tabG = smem + 40 * 1024;
  const int nl = tid & 127;
  STAMP(2, 0);
  u16 in_la[2], in_ai[2], in_cr[2], in_ci[2];
  const u16 in_dt = logdt[d];
#pragma unroll
  for (int pass = 0; pass < 2; ++pass) {
    in_la[pass] = logA[d * NN + pass * 128 + nl];
    in_ai[pass] = Aim[d * NN + pass * 128 + nl];
    in_cr[pass] = Cre[d * NN + pass * 128 + nl];
    in_ci[pass] = Cim[d * NN + pass * 128 + nl];
  }

  const float dt = rbf(expf(bf2f(in_dt)));
  STAMPV(2, 1, dt);
  f32x4 acc = {0.f, 0.f, 0.f, 0.f};
  float last = 0.f;
  const int seg = wave & 3, kh = wave >> 2;
  const int fi = lane & 15, fq = lane >> 4;
  int gidx, eidx;
  if (seg == 0) { gidx = fi; eidx = fi; }
  else {
    gidx = 8 + 8 * seg + (fi & 7);
    int e = fi << seg;
    eidx = e < 16 ? e : (e < 32 ? 16 + ((e - 16) >> 1) : (e < 64 ? 24 + ((e - 32) >> 2) : 32 + ((e - 64) >> 3)));
  }
  const int gk = gkey(gidx), ek = ekey(eidx);
  const int swb = ((nl >> 1) << 4) + ((nl & 1) << 3);

#pragma unroll 1
  for (int pass = 0; pass < 2; ++pass) {
    {
      const u16 p_la = pass ? in_la[1] : in_la[0], p_ai = pass ? in_ai[1] : in_ai[0];
      const u16 p_cr = pass ? in_cr[1] : in_cr[0], p_ci = pass ? in_ci[1] : in_ci[0];
      float ea = rbf(expf(bf2f(p_la)));
      const bool isG = role >= 2;
      const float sc = isG ? 16.f : 1.f, sg = isG ? -1.f : 1.f;
      const float are = (-ea * dt) * sc;
      const float aim = (bf2f(p_ai) * dt) * (sc * sg);
      f2 c = {1.f, 0.f};
      if (isG) { c.x = bf2f(p_cr) * dt; c.y = -(bf2f(p_ci) * dt); }
      char* tab = isG ? tabG : tabE;
      const unsigned long long keys_lo = isG ? (role & 1 ? GK_HI0 : GK_LO0) : (role & 1 ? EK_HI0 : EK_LO0);
      const unsigned keys_hi = isG ? (role & 1 ? GK_HI1 : GK_LO1) : (role & 1 ? EK_HI1 : EK_LO1);
#pragma unroll 1
      for (int sgi = 0; sgi < 5; ++sgi) {
        const int sgp = (role & 1) * 5 + sgi;
        const float e0f = (float)((sgp < 4) ? 4 * sgp : (sgp < 6 ? 16 + 8 * (sgp - 4) : (sgp < 8 ? 32 + 16 * (sgp - 6) : 64 + 32 * (sgp - 8))));
        const float stf = (float)(sgp < 4 ? 1 : (sgp < 6 ? 2 : (sgp < 8 ? 4 : 8)));
        f2 v = cmul(c, cexp_(are * e0f, aim * e0f));
        const f2 st = cexp_(are * stf, aim * stf);
#pragma unroll
        for (int j = 0; j < 4; ++j) {
          const int li = sgi * 4 + j;
          const int key = (int)((li < 16 ? (keys_lo >> (4 * li)) : (unsigned long long)(keys_hi >> (4 * (li - 16)))) & 15ull);
          kg_store(tab, swb, sgp * 4 + j, key, v);
          if (j < 3) v = cmul(v, st);
        }
      }
      if (role == 3) last += cmul(c, cexp_(are * 128.f, aim * 128.f)).x;
    }
    STAMP(2, 2 + pass * 3);
    __syncthreads();
    STAMP(2, 3 + pass * 3);
    if (pass == 0) STAMP(1, 3);
#pragma unroll
    for (int kb = 0; kb < 2; ++kb) {
      uint4 g[4], e[4];
#pragma unroll
      for (int ks = 0; ks < 4; ++ks) {
        int chunk = (kh * 8 + kb * 4 + ks) * 4 + fq;
        g[ks] = *(const uint4*)(tabG + gidx * 1024 + ((chunk ^ gk) << 4));
        e[ks] = *(const uint4*)(tabE + eidx * 1024 + ((chunk ^ ek) << 4));
      }
      SB();
#pragma unroll
      for (int ks = 0; ks < 4; ++ks) {
        uint4 es = make_uint4(e[ks].y, e[ks].x, e[ks].w, e[ks].z);
        acc = __builtin_amdgcn_mfma_f32_16x16x32_bf16(__builtin_bit_cast(bf16x8, g[ks]), __builtin_bit_cast(bf16x8, es), acc, 0, 0, 0);
        acc = __builtin_amdgcn_mfma_f32_16x16x32_bf16(__builtin_bit_cast(bf16x8, g[ks]), __builtin_bit_cast(bf16x8, e[ks]), acc, 0, 0, 0);
      }
      SB();
    }
    __syncthreads();
    STAMP(2, 4 + pass * 3);
  }
  float* Kd = (float*)(smem + 74752);
  float* red = Kd + 1296;
  last = wave_sum(last);
  if (lane == 0) red[wave] = last;
#pragma unroll
  for (int r = 0; r < 4; ++r) {
    int row = fq * 4 + r;
    if (seg == 0) Kd[kh * 648 + row * 16 + fi] = acc[r];
    else if (row < 8) Kd[kh * 648 + 256 + (seg - 1) * 128 + row * 16 + fi] = acc[r];
  }
  __syncthreads();
  {
    const int g = tid >> 1, hf = tid & 1;
    u32 wh[2], wl[2];
#pragma unroll
    for (int jj = 0; jj < 2; ++jj) {
      f2 kv;
#pragma unroll
      for (int e = 0; e < 2; ++e) {
        int l = 8 * g + 7 - (4 * hf + 2 * jj + e);
        int p = (int)rbf((float)l);
        int idx;
        if (p < 256) idx = p;
        else {
          int s = (31 - __clz(p)) - 7;
          idx = 256 + (s - 1) * 128 + ((p >> s) - 128);
        }
        int ix = idx < 640 ? idx : 0;
        float v = Kd[ix] + Kd[648 + ix];
        if (idx >= 640) v = red[6] + red[7];
        kv[e] = 2.f * v;
      }
      bf2 hq = __builtin_convertvector(kv, bf2);
      bf2 lq = __builtin_convertvector(kv - __builtin_convertvector(hq, f2), bf2);
      wh[jj] = __builtin_bit_cast(u32, hq);
      wl[jj] = __builtin_bit_cast(u32, lq);
    }
    char* ko = (char*)(Kp + (size_t)d * 2 * KPAD);
    *(uint2*)(ko + 512 + 8 * tid) = make_uint2(wh[0], wh[1]);
    *(uint2*)(ko + 4608 + 512 + 8 * tid) = make_uint2(wl[0], wl[1]);
    if (tid < 32) {
      *(uint4*)(ko + 16 * tid) = make_uint4(0, 0, 0, 0);
      *(uint4*)(ko + 4608 + 16 * tid) = make_uint4(0, 0, 0, 0);
    }
  }
  STAMP(2, 8);
  if (has_tr) {
    u16* tile = (u16*)smem;
    u32* tw = (u32*)(tile + (tid >> 3) * 66 + (tid & 7) * 8);
    tw[0] = tr_raw.x; tw[1] = tr_raw.y; tw[2] = tr_raw.z; tw[3] = tr_raw.w;
    __syncthreads();
    const int c = tid >> 3, r8 = tid & 7;
    u32 w[4];
#pragma unroll
    for (int j = 0; j < 4; ++j)
      w[j] = (u32)tile[(r8 * 8 + 2 * j) * 66 + c] | ((u32)tile[(r8 * 8 + 2 * j + 1) * 66 + c] << 16);
    *(uint4*)(tr_out + (size_t)(tr_c * 64 + c) * trR + tr_r * 64 + r8 * 8) = make_uint4(w[0], w[1], w[2], w[3]);
  }
}

__device__ void ln_role(int blk, const u16* __restrict__ x, u16* __restrict__ xn, u16* __restrict__ xnT, char* smem) {
  const int tid = threadIdx.x, wave = tid >> 6, lane = tid & 63;
  u16* tile = (u16*)smem;
  const int row = wave * 8 + (lane >> 3), c = lane & 7;
  const int l = blk * 64 + row;
  STAMP(0, 0);
  uint4 raw[7];
#pragma unroll
  for (int j = 0; j < 7; ++j) raw[j] = *(const uint4*)(x + (size_t)l * DD + j * 64 + c * 8);
  float s = 0.f;
#pragma unroll
  for (int j = 0; j < 7; ++j) {
    s += (lo16f(raw[j].x) + hi16f(raw[j].x)) + (lo16f(raw[j].y) + hi16f(raw[j].y));
    s += (lo16f(raw[j].z) + hi16f(raw[j].z)) + (lo16f(raw[j].w) + hi16f(raw[j].w));
  }
  s += __shfl_xor(s, 1); s += __shfl_xor(s, 2); s += __shfl_xor(s, 4);
  const float mean = s * (1.f / DD);
  STAMP(0, 1);
  float q = 0.f;
#pragma unroll
  for (int j = 0; j < 7; ++j) {
    u32 w4[4] = {raw[j].x, raw[j].y, raw[j].z, raw[j].w};
#pragma unroll
    for (int e = 0; e < 4; ++e) {
      float t0 = lo16f(w4[e]) - mean, t1 = hi16f(w4[e]) - mean;
      q = fmaf(t0, t0, q); q = fmaf(t1, t1, q);
    }
  }
  q += __shfl_xor(q, 1); q += __shfl_xor(q, 2); q += __shfl_xor(q, 4);
  const float var = q * (1.f / DD);
  const float mb = rbf(mean), vb = rbf(var);
  const float den = rbf(sqrtf(rbf(vb + 1e-5f)));
  const float rden = 1.0f / den;
#pragma unroll
  for (int j = 0; j < 7; ++j) {
    u32 w4[4] = {raw[j].x, raw[j].y, raw[j].z, raw[j].w};
    u32 o[4];
#pragma unroll
    for (int e = 0; e < 4; ++e)
      o[e] = pack2(fdiv(rbf(lo16f(w4[e]) - mb), den, rden), fdiv(rbf(hi16f(w4[e]) - mb), den, rden));
    *(uint4*)(xn + (size_t)l * DD + j * 64 + c * 8) = make_uint4(o[0], o[1], o[2], o[3]);
    u32* trow = (u32*)(tile + row * 450 + j * 64 + c * 8);
    trow[0] = o[0]; trow[1] = o[1]; trow[2] = o[2]; trow[3] = o[3];
  }
  STAMP(0, 2);
  __syncthreads();
  STAMP(0, 3);
  for (int v = tid; v < 2 * DD; v += 512) {
    const int d = v >> 1, rh = (v & 1) * 32;
    u32 w[16];
#pragma unroll
    for (int r = 0; r < 16; ++r)
      w[r] = (u32)tile[(rh + 2 * r) * 450 + d] | ((u32)tile[(rh + 2 * r + 1) * 450 + d] << 16);
    uint4* dst = (uint4*)(xnT + (size_t)d * LL + blk * 64 + rh);
    dst[0] = make_uint4(w[0], w[1], w[2], w[3]);
    dst[1] = make_uint4(w[4], w[5], w[6], w[7]);
    dst[2] = make_uint4(w[8], w[9], w[10], w[11]);
    dst[3] = make_uint4(w[12], w[13], w[14], w[15]);
  }
  STAMP(0, 4);
}

__device__ void tr_role(const u16* __restrict__ in, u16* __restrict__ out, int R, int C, int tr, int tc, char* smem) {
  const int tid = threadIdx.x;
  u16* tile = (u16*)smem;
  __syncthreads();
  {
    int v = tid;
    int r = v >> 3, c8 = v & 7;
    uint4 raw = *(const uint4*)(in + (size_t)(tr * 64 + r) * C + tc * 64 + c8 * 8);
    u32* t = (u32*)(tile + r * 66 + c8 * 8);
    t[0] = raw.x; t[1] = raw.y; t[2] = raw.z; t[3] = raw.w;
  }
  __syncthreads();
  {
    int v = tid;
    int c = v >> 3, r8 = v & 7;
    u32 w[4];
#pragma unroll
    for (int j = 0; j < 4; ++j)
      w[j] = (u32)tile[(r8 * 8 + 2 * j) * 66 + c] | ((u32)tile[(r8 * 8 + 2 * j + 1) * 66 + c] << 16);
    *(uint4*)(out + (size_t)(tc * 64 + c) * R + tr * 64 + r8 * 8) = make_uint4(w[0], w[1], w[2], w[3]);
  }
}

__global__ __launch_bounds__(512, 4) void k1_prep(const u16* __restrict__ x, const u16* __restrict__ logA,
                                                  const u16* __restrict__ Aim, const u16* __restrict__ Cre,
                                                  const u16* __restrict__ Cim, const u16* __restrict__ logdt,
                                                  const u16* __restrict__ w1, const u16* __restrict__ w2, char* ws) {
  __shared__ __attribute__((aligned(16))) char smem[81920];
  const int b = blockIdx.x;
  if (b < 32) {
    if (b == 0 && threadIdx.x < 32) *(u32*)(ws + WS_CNT + threadIdx.x * 128) = 0u;
    ln_role(b, x, (u16*)(ws + WS_XN), (u16*)(ws + WS_XNT), smem);
  } else {
    kgen_role(b - 32, logA, Aim, Cre, Cim, logdt, (u16*)(ws + WS_KP), w1, w2, (u16*)(ws + WS_W1T),
              (u16*)(ws + WS_W2T), smem);
  }
}

#define RLX_AGENT __ATOMIC_RELAXED, __HIP_MEMORY_SCOPE_AGENT
typedef __attribute__((address_space(1))) unsigned gu32;
typedef __attribute__((ext_vector_type(4))) unsigned v4u;

__device__ __forceinline__ void wait_count(u32* word, unsigned want) {
  for (unsigned spins = 0; spins < (1u << 22); ++spins) {
    unsigned v = __builtin_amdgcn_readfirstlane(__hip_atomic_load(word, RLX_AGENT));
    if (v >= want) break;
    __builtin_amdgcn_s_sleep(1);
  }
}

__device__ __forceinline__ void x_role(int blk, char* ws, const u16* __restrict__ b1, const u16* __restrict__ x,
                                       const u16* __restrict__ b2, u16* __restrict__ out, char* smem) {
  const int tid = threadIdx.x, wave = tid >> 6, lane = tid & 63;
  const int xcd = blk & 7, idx = blk >> 3;
  const int r32 = lane & 31, hh = lane >> 5;
  const u16* xn = (const u16*)(ws + WS_XN);
  const u16* w1T = (const u16*)(ws + WS_W1T);
  const u16* w2T = (const u16*)(ws + WS_W2T);
  u16* h = (u16*)(ws + WS_H);
  const u16* sT = (const u16*)(ws + WS_ST);
  u32* cnt = (u32*)(ws + WS_CNT);
  f32x16 acc[2];
  {
    const int mt = xcd * 2 + (idx & 1), nt = idx >> 1;
    const int l0 = mt * 128, f0 = nt * 128;
    const int wr = wave >> 2, wc = wave & 3;
    const u16* srcA;
    const u16* srcB;
    {
      int row = wave * 16 + (lane >> 2);
      int src = (lane & 3) ^ ((row >> 2) & 3);
      srcA = w1T + (size_t)(f0 + row) * DD + src * 8;
      srcB = xn + (size_t)(l0 + row) * DD + src * 8;
    }
    auto gls = [&](int t, int slot, int q) {
      char* dst = smem + slot * 16384 + q * 8192 + wave * 1024 + lane * 16;
      const u16* sp = (q ? srcB : srcA) + t * 32;
      __builtin_amdgcn_global_load_lds((const u32*)sp, (u32*)dst, 16, 0, 0);
    };
    auto gl = [&](int t, int q) { gls(t, t % 5, q); };
    int fo[3], fs[3];
#pragma unroll
    for (int w = 0; w < 3; ++w) {
      int row = (w < 2) ? (wr * 64 + w * 32 + r32) : (wc * 32 + r32);
      fo[w] = (w == 2 ? 8192 : 0) + row * 64;
      fs[w] = (row >> 2) & 3;
    }
#pragma unroll
    for (int i = 0; i < 2; ++i)
#pragma unroll
      for (int r = 0; r < 16; ++r) acc[i][r] = 0.f;
    STAMP(1, 0);
    uint2 braw_[2][4];
#pragma unroll
    for (int i = 0; i < 2; ++i)
#pragma unroll
      for (int g = 0; g < 4; ++g) braw_[i][g] = *(const uint2*)(b1 + f0 + wr * 64 + i * 32 + 8 * g + 4 * hh);
#pragma unroll
    for (int t = 0; t < 4; ++t) { gl(t, 0); gl(t, 1); }
    auto step1 = [&](int t, int slot, int slot4, bool issue) {
      const char* bs = smem + slot * 16384;
      bf16x8 fr[2][3];
#pragma unroll
      for (int kk = 0; kk < 2; ++kk)
#pragma unroll
        for (int w = 0; w < 3; ++w) fr[kk][w] = *(const bf16x8*)(bs + fo[w] + (((2 * kk + hh) ^ fs[w]) << 4));
      SB();
#pragma unroll
      for (int kk = 0; kk < 2; ++kk) {
        if (issue) gls(t + 4, slot4, kk);
        acc[0] = __builtin_amdgcn_mfma_f32_32x32x16_bf16(fr[kk][0], fr[kk][2], acc[0], 0, 0, 0);
        acc[1] = __builtin_amdgcn_mfma_f32_32x32x16_bf16(fr[kk][1], fr[kk][2], acc[1], 0, 0, 0);
        SB();
      }
    };
    {
      int slot = 0;
#pragma unroll 1
      for (int t = 0; t < 10; ++t) {
        asm volatile("s_waitcnt vmcnt(6) lgkmcnt(0)" ::: "memory");
        __builtin_amdgcn_s_barrier();
        step1(t, slot, slot == 0 ? 4 : slot - 1, true);
        slot = slot == 4 ? 0 : slot + 1;
      }
      asm volatile("s_waitcnt vmcnt(6) lgkmcnt(0)" ::: "memory");
      __builtin_amdgcn_s_barrier();
      step1(10, 0, 0, false);
      asm volatile("s_waitcnt vmcnt(4) lgkmcnt(0)" ::: "memory");
      __builtin_amdgcn_s_barrier();
      step1(11, 1, 0, false);
      asm volatile("s_waitcnt vmcnt(2) lgkmcnt(0)" ::: "memory");
      __builtin_amdgcn_s_barrier();
      step1(12, 2, 0, false);
      asm volatile("s_waitcnt vmcnt(0) lgkmcnt(0)" ::: "memory");
      __builtin_amdgcn_s_barrier();
      step1(13, 3, 0, false);
    }
    STAMP(1, 1);
    __syncthreads();
    {
      const int ll = wc * 32 + r32;
#pragma unroll
      for (int i = 0; i < 2; ++i)
#pragma unroll
        for (int g = 0; g < 4; ++g) {
          int fl = wr * 64 + i * 32 + 8 * g + 4 * hh;
          uint2 braw = braw_[i][g];
          f2 bb0 = {lo16f(braw.x), hi16f(braw.x)}, bb1 = {lo16f(braw.y), hi16f(braw.y)};
          f2 a0 = {acc[i][4 * g], acc[i][4 * g + 1]}, a1 = {acc[i][4 * g + 2], acc[i][4 * g + 3]};
          *(uint2*)(smem + ll * 256 + (((fl >> 3) ^ (ll & 15)) << 4) + (fl & 4) * 2) =
              make_uint2(sqrelu2(a0, bb0), sqrelu2(a1, bb1));
        }
    }
    __syncthreads();
    {
      const __amdgpu_buffer_rsrc_t hr = __builtin_amdgcn_make_buffer_rsrc((void*)h, 0, LL * FF * 2, 0x00020000);
#pragma unroll
      for (int i = 0; i < 4; ++i) {
        int row = i * 32 + (tid >> 4), ch = tid & 15;
        v4u v = *(const v4u*)(smem + row * 256 + ((ch ^ (row & 15)) << 4));
        __builtin_amdgcn_raw_buffer_store_b128(v, hr, (unsigned)(((l0 + row) * FF + f0 + ch * 8) * 2), 0, 16);
      }
    }
    asm volatile("s_waitcnt vmcnt(0)" ::: "memory");
    __syncthreads();
    if (tid == 0) __hip_atomic_fetch_add(cnt + mt * 32, 1u, RLX_AGENT);
    STAMP(1, 2);
  }
  {
    const int mt = xcd * 4 + (idx & 3), nt = idx >> 2;
    const int l0 = mt * 64, d0 = nt * 64;
    const int kq = wave & 3, wi = wave >> 2;
    const u16* srcA;
    const u16* srcB;
    {
      int row = wave * 8 + (lane >> 3);
      int src = (lane & 7) ^ ((row >> 1) & 7);
      srcA = w2T + (size_t)(d0 + row) * FF + src * 8;
      srcB = h + (size_t)(l0 + row) * FF + src * 8;
    }
    auto gls = [&](int t, int slot, int q) {
      char* dst = smem + slot * 16384 + q * 8192 + wave * 1024 + lane * 16;
      const u16* sp = (q ? srcB : srcA) + t * 64;
      __builtin_amdgcn_global_load_lds((const u32*)sp, (u32*)dst, 16, 0, 0);
    };
    auto gl = [&](int t, int q) { gls(t, t % 5, q); };
    const int quad = wave >> 1, half = wave & 1;
    const int er = quad >> 1, ec = quad & 1;
    const int l = l0 + ec * 32 + r32;
    uint2 braw_[2], xraw_[2];
#pragma unroll
    for (int g2 = 0; g2 < 2; ++g2) {
      int d = d0 + er * 32 + 8 * (half * 2 + g2) + 4 * hh;
      braw_[g2] = *(const uint2*)(b2 + d);
      xraw_[g2] = *(const uint2*)(x + (size_t)l * DD + d);
    }
#pragma unroll
    for (int t = 0; t < 4; ++t) gl(t, 0);
    if (wave == 0) {
      wait_count(cnt + (mt >> 1) * 32, 14u);
      __builtin_amdgcn_fence(__ATOMIC_ACQUIRE, "agent");
      asm volatile("s_waitcnt vmcnt(0)" ::: "memory");
    }
    __syncthreads();
    STAMP(1, 3);
#pragma unroll
    for (int i = 0; i < 2; ++i)
#pragma unroll
      for (int r = 0; r < 16; ++r) acc[i][r] = 0.f;
#pragma unroll
    for (int t = 0; t < 4; ++t) gl(t, 1);
    int fo[3];
#pragma unroll
    for (int w = 0; w < 3; ++w) {
      int row = (w == 0) ? (wi * 32 + r32) : ((w - 1) * 32 + r32);
      int ch = 2 * kq + hh;
      fo[w] = (w ? 8192 : 0) + row * 128 + ((ch ^ ((row >> 1) & 7)) << 4);
    }
    auto step2 = [&](int t, int slot, int slot4, bool issue) {
      const char* bs = smem + slot * 16384;
      bf16x8 fr[3];
#pragma unroll
      for (int w = 0; w < 3; ++w) fr[w] = *(const bf16x8*)(bs + fo[w]);
      SB();
      if (issue) gls(t + 4, slot4, 0);
      acc[0] = __builtin_amdgcn_mfma_f32_32x32x16_bf16(fr[0], fr[1], acc[0], 0, 0, 0);
      SB();
      if (issue) gls(t + 4, slot4, 1);
      acc[1] = __builtin_amdgcn_mfma_f32_32x32x16_bf16(fr[0], fr[2], acc[1], 0, 0, 0);
      SB();
    };
    {
      asm volatile("s_waitcnt vmcnt(3) lgkmcnt(0)" ::: "memory");
      __builtin_amdgcn_s_barrier();
      step2(0, 0, 4, true);
      asm volatile("s_waitcnt vmcnt(4) lgkmcnt(0)" ::: "memory");
      __builtin_amdgcn_s_barrier();
      step2(1, 1, 0, true);
      asm volatile("s_waitcnt vmcnt(5) lgkmcnt(0)" ::: "memory");
      __builtin_amdgcn_s_barrier();
      step2(2, 2, 1, true);
      int slot = 3;
#pragma unroll 1
      for (int t = 3; t < 24; ++t) {
        asm volatile("s_waitcnt vmcnt(6) lgkmcnt(0)" ::: "memory");
        __builtin_amdgcn_s_barrier();
        step2(t, slot, slot == 0 ? 4 : slot - 1, true);
        slot = slot == 4 ? 0 : slot + 1;
      }
      asm volatile("s_waitcnt vmcnt(6) lgkmcnt(0)" ::: "memory");
      __builtin_amdgcn_s_barrier();
      step2(24, 4, 0, false);
      asm volatile("s_waitcnt vmcnt(4) lgkmcnt(0)" ::: "memory");
      __builtin_amdgcn_s_barrier();
      step2(25, 0, 0, false);
      asm volatile("s_waitcnt vmcnt(2) lgkmcnt(0)" ::: "memory");
      __builtin_amdgcn_s_barrier();
      step2(26, 1, 0, false);
      asm volatile("s_waitcnt vmcnt(0) lgkmcnt(0)" ::: "memory");
      __builtin_amdgcn_s_barrier();
      step2(27, 2, 0, false);
    }
    STAMP(1, 4);
    __syncthreads();
    float* red = (float*)smem;
#pragma unroll
    for (int j = 0; j < 2; ++j)
#pragma unroll
      for (int r4 = 0; r4 < 4; ++r4)
        *(float4*)(red + ((kq * 4 + wi * 2 + j) * 4 + r4) * 256 + lane * 4) =
            make_float4(acc[j][4 * r4], acc[j][4 * r4 + 1], acc[j][4 * r4 + 2], acc[j][4 * r4 + 3]);
    if (wave == 0) wait_count(cnt + (16 + nt) * 32, 64u);
    __syncthreads();
    STAMP(1, 5);
    u16 sraw_[8];
#pragma unroll
    for (int g2 = 0; g2 < 2; ++g2) {
      int d = d0 + er * 32 + 8 * (half * 2 + g2) + 4 * hh;
#pragma unroll
      for (int e = 0; e < 4; ++e) sraw_[4 * g2 + e] = __hip_atomic_load(sT + (size_t)(d + e) * LL + l, RLX_AGENT);
    }
    float fin[8];
#pragma unroll
    for (int r = 0; r < 8; ++r) fin[r] = 0.f;
#pragma unroll
    for (int w = 0; w < 4; ++w)
#pragma unroll
      for (int g2 = 0; g2 < 2; ++g2) {
        float4 v = *(const float4*)(red + ((w * 4 + quad) * 4 + half * 2 + g2) * 256 + lane * 4);
        fin[4 * g2] += v.x; fin[4 * g2 + 1] += v.y; fin[4 * g2 + 2] += v.z; fin[4 * g2 + 3] += v.w;
      }
#pragma unroll
    for (int g2 = 0; g2 < 2; ++g2) {
      int d = d0 + er * 32 + 8 * (half * 2 + g2) + 4 * hh;
      uint2 braw = braw_[g2];
      uint2 xraw = xraw_[g2];
      float bb[4] = {lo16f(braw.x), hi16f(braw.x), lo16f(braw.y), hi16f(braw.y)};
      float xx[4] = {lo16f(xraw.x), hi16f(xraw.x), lo16f(xraw.y), hi16f(xraw.y)};
      float o[4];
#pragma unroll
      for (int e = 0; e < 4; ++e) {
        float v = rbf(rbf(fin[4 * g2 + e]) + bb[e]);
        float gg = rbf(v * __builtin_amdgcn_rcpf(1.f + __builtin_amdgcn_exp2f(v * -1.44269504088896340736f)));
        float s = bf2f(sraw_[4 * g2 + e]);
        o[e] = xx[e] + rbf(s * gg);
      }
      *(uint2*)(out + (size_t)l * DD + d) = make_uint2(pack2(o[0], o[1]), pack2(o[2], o[3]));
    }
    STAMP(1, 6);
  }
}

#define CV_KOFF 0
#define CV_WOFF 9216
#define CV_PHIP 5136
#define CV_ZOFF (CV_WOFF + 8 * CV_PHIP)
#define CV_KD 74752
__device__ __forceinline__ void conv_one(int d, uint4 k0, uint4 k1, uint4 cur, uint4 prv, u16 dkraw, bool drain,
                                         u16* __restrict__ sT, char* smem) {
  int tid = threadIdx.x;
  asm volatile("" : "+v"(tid));
  const int wave = tid >> 6, lane = tid & 63;
  const int t2 = tid & 255;
  STAMP(1, 0);
  if (t2 == 0) prv = make_uint4(0, 0, 0, 0);
  {
    uint4* dst = (uint4*)(smem + CV_KOFF);
    dst[tid] = k0;
    if (tid < 64) dst[tid + 512] = k1;
  }
  if (tid >= 256) {
    u32 Dw[8] = {prv.x, prv.y, prv.z, prv.w, cur.x, cur.y, cur.z, cur.w};
    char* wb = smem + CV_WOFF + (t2 >> 5) * 640 + (t2 & 31) * 16;
#pragma unroll
    for (int phi = 0; phi < 8; ++phi) {
      int e = phi + 1;
      int k = e >> 1;
      uint4 o;
      if ((e & 1) == 0) {
        o = make_uint4(Dw[k], Dw[k + 1], Dw[k + 2], Dw[k + 3]);
      } else {
        o.x = __builtin_amdgcn_alignbit(Dw[k + 1], Dw[k], 16);
        o.y = __builtin_amdgcn_alignbit(Dw[k + 2], Dw[k + 1], 16);
        o.z = __builtin_amdgcn_alignbit(Dw[k + 3], Dw[k + 2], 16);
        o.w = __builtin_amdgcn_alignbit(Dw[k + 4], Dw[k + 3], 16);
      }
      *(uint4*)(wb + phi * CV_PHIP) = o;
    }
    if (t2 == 0) *(uint4*)(smem + CV_ZOFF) = make_uint4(0, 0, 0, 0);
  }
  __syncthreads();
  STAMP(1, 1);
  const int r32 = lane & 31, hh = lane >> 5;
  const int phi = r32 & 7, al = r32 >> 3;
  f32x16 acc0, acc1;
#pragma unroll
  for (int r = 0; r < 16; ++r) { acc0[r] = 0.f; acc1[r] = 0.f; }
  {
    bf16x8 fr[2][6];
    auto rd = [&](int g, int w) -> bf16x8 {
      int dl = g >> 1, tt = g & 1;
      int v = 2 * (wave * 2 + tt) + hh;
      int off;
      if (w < 4) {
        off = CV_KOFF + 512 + 512 * dl + 16 * (r32 - v) + (w & 1) * 4608 + (w >> 1) * 2048;
      } else {
        int ci = al - dl + (w - 4) * 4;
        off = ci < 0 ? CV_ZOFF : (CV_WOFF + phi * CV_PHIP + ci * 640 + v * 16);
      }
      return *(const bf16x8*)(smem + off);
    };
#pragma unroll
    for (int w = 0; w < 6; ++w) fr[0][w] = rd(0, w);
    SB();
#pragma unroll
    for (int g = 0; g < 8; ++g) {
      const int c = g & 1, n = c ^ 1;
      acc0 = __builtin_amdgcn_mfma_f32_32x32x16_bf16(fr[c][1], fr[c][4], acc0, 0, 0, 0);
      if (g < 7) fr[n][4] = rd(g + 1, 4);
      SB();
      acc1 = __builtin_amdgcn_mfma_f32_32x32x16_bf16(fr[c][3], fr[c][4], acc1, 0, 0, 0);
      if (g < 7) fr[n][1] = rd(g + 1, 1);
      SB();
      acc0 = __builtin_amdgcn_mfma_f32_32x32x16_bf16(fr[c][0], fr[c][4], acc0, 0, 0, 0);
      if (g < 7) fr[n][3] = rd(g + 1, 3);
      SB();
      acc1 = __builtin_amdgcn_mfma_f32_32x32x16_bf16(fr[c][2], fr[c][4], acc1, 0, 0, 0);
      if (g < 7) fr[n][0] = rd(g + 1, 0);
      SB();
      acc1 = __builtin_amdgcn_mfma_f32_32x32x16_bf16(fr[c][1], fr[c][5], acc1, 0, 0, 0);
      if (g < 7) fr[n][2] = rd(g + 1, 2);
      SB();
      acc1 = __builtin_amdgcn_mfma_f32_32x32x16_bf16(fr[c][0], fr[c][5], acc1, 0, 0, 0);
      if (g < 7) fr[n][5] = rd(g + 1, 5);
      SB();
    }
  }
  STAMP(1, 2);
  __syncthreads();
  float* Y = (float*)(smem + CV_WOFF);
#pragma unroll
  for (int reg = 0; reg < 16; ++reg) {
    int r = (reg & 3) + 8 * (reg >> 2) + 4 * hh;
    int l = 8 * r + phi + 256 * al;
    Y[wave * 2048 + l] = acc0[reg];
    Y[wave * 2048 + l + 1024] = acc1[reg];
  }
  __syncthreads();
  if (tid >= 256) {
    float y8[8];
#pragma unroll
    for (int j = 0; j < 8; ++j) y8[j] = 0.f;
#pragma unroll
    for (int w = 0; w < 8; ++w) {
      const float4* p = (const float4*)(Y + w * 2048 + 8 * t2);
      float4 a = p[0], b = p[1];
      y8[0] += a.x; y8[1] += a.y; y8[2] += a.z; y8[3] += a.w;
      y8[4] += b.x; y8[5] += b.y; y8[6] += b.z; y8[7] += b.w;
    }
    float dk = bf2f(dkraw);
    u32 cw[4] = {cur.x, cur.y, cur.z, cur.w};
    u32 o[4];
#pragma unroll
    for (int j = 0; j < 4; ++j) {
      float s0 = rbf(y8[2 * j]) + rbf(lo16f(cw[j]) * dk);
      float s1 = rbf(y8[2 * j + 1]) + rbf(hi16f(cw[j]) * dk);
      o[j] = pack2(s0, s1);
    }
    const __amdgpu_buffer_rsrc_t sr = __builtin_amdgcn_make_buffer_rsrc((void*)sT, 0, LL * DD * 2, 0x00020000);
    v4u ov = {o[0], o[1], o[2], o[3]};
    __builtin_amdgcn_raw_buffer_store_b128(ov, sr, (unsigned)((d * LL + 8 * t2) * 2), 0, 16);
    if (drain) asm volatile("s_waitcnt vmcnt(0)" ::: "memory");
  }
  __syncthreads();
  STAMP(1, 3);
}

__device__ __forceinline__ void conv_role(int y, const u16* __restrict__ xnT, const u16* __restrict__ Kp,
                                          const u16* __restrict__ Dsk, u16* __restrict__ sT, u32* cnt, char* smem) {
  const int tid0 = threadIdx.x;
  const int t2 = tid0 & 255;
  const int dA = y, dB = y + 224;
  const uint4* kA = (const uint4*)(Kp + (size_t)dA * 2 * KPAD);
  const uint4* kB = (const uint4*)(Kp + (size_t)dB * 2 * KPAD);
  const uint4* uA = (const uint4*)(xnT + (size_t)dA * LL);
  const uint4* uB = (const uint4*)(xnT + (size_t)dB * LL);
  const int i1 = tid0 < 64 ? tid0 + 512 : tid0, ip = t2 > 0 ? t2 - 1 : 0;
  uint4 a0 = kA[tid0], a1 = kA[i1], ac = uA[t2], ap = uA[ip];
  u16 ad = Dsk[dA];
  uint4 b0 = kB[tid0], b1 = kB[i1], bc = uB[t2], bp = uB[ip];
  u16 bd = Dsk[dB];
  conv_one(dA, a0, a1, ac, ap, ad, false, sT, smem);
  conv_one(dB, b0, b1, bc, bp, bd, true, sT, smem);
  if (tid0 == 0) {
    __hip_atomic_fetch_add(cnt + (16 + (dA >> 6)) * 32, 1u, RLX_AGENT);
    __hip_atomic_fetch_add(cnt + (16 + (dB >> 6)) * 32, 1u, RLX_AGENT);
  }
  STAMP(1, 4);
}

__global__ __launch_bounds__(512, 4) void k2_flow(const u16* __restrict__ b1, const u16* __restrict__ Dsk,
                                                  const u16* __restrict__ x, const u16* __restrict__ b2,
                                                  char* ws, u16* __restrict__ out) {
  __shared__ __attribute__((aligned(16))) char smem[81920];
  const int b = blockIdx.x;
  if (b < 224) {
    x_role(b, ws, b1, x, b2, out, smem);
  } else {
    conv_role(b - 224, (const u16*)(ws + WS_XNT), (const u16*)(ws + WS_KP), Dsk, (u16*)(ws + WS_ST),
              (u32*)(ws + WS_CNT), smem);
  }
}

extern "C" void kernel_launch(void* const* d_in, const int* in_sizes, int n_in,
                              void* d_out, int out_size, void* d_ws, size_t ws_size,
                              hipStream_t stream) {
  const u16* x = (const u16*)d_in[0];
  const u16* logA = (const u16*)d_in[1];
  const u16* Aim = (const u16*)d_in[2];
  const u16* Cre = (const u16*)d_in[3];
  const u16* Cim = (const u16*)d_in[4];
  const u16* logdt = (const u16*)d_in[5];
  const u16* Dsk = (const u16*)d_in[6];
  const u16* w1 = (const u16*)d_in[8];
  const u16* b1 = (const u16*)d_in[9];
  const u16* w2 = (const u16*)d_in[10];
  const u16* b2 = (const u16*)d_in[11];
  char* ws = (char*)d_ws;
  k1_prep<<<32 + DD, 512, 0, stream>>>(x, logA, Aim, Cre, Cim, logdt, w1, w2, ws);
  k2_flow<<<448, 512, 0, stream>>>(b1, Dsk, x, b2, ws, (u16*)d_out);
}
```

```cpp
#include <hip/hip_runtime.h>
#include <hip/hip_bf16.h>
#include <stdint.h>

typedef __attribute__((ext_vector_type(8))) short bf16x8;
typedef __attribute__((ext_vector_type(4))) float f32x4;
typedef __attribute__((ext_vector_type(16))) float f32x16;
typedef unsigned int u32;
typedef unsigned short u16;

#define LL 2048
#define DD 448
#define NN 256
#define FF 1792
#define KPAD 2304

#define WS_XN 0
#define WS_XNT (WS_XN + LL * DD * 2)
#define WS_W1T (WS_XNT + LL * DD * 2)
#define WS_W2T (WS_W1T + DD * FF * 2)
#define WS_KP (WS_W2T + DD * FF * 2)
#define WS_H (WS_KP + DD * 2 * KPAD * 2)
#define WS_ST (WS_H + LL * FF * 2)
#define WS_CNT (WS_ST + LL * DD * 2)
#define WS_END (WS_CNT + 32 * 128)

#define STAMP(K, I)
#define STAMPV(K, I, V)
#define RSTAMP(K, I)
typedef __attribute__((ext_vector_type(4))) unsigned v4u_;
typedef __attribute__((ext_vector_type(2))) unsigned v2u_;
#define STMODE 2
__device__ __forceinline__ void st16(void* p, uint4 v) {
  asm volatile("global_store_dwordx4 %0, %1, off sc1\n\ts_nop 1" ::"v"(p), "v"((v4u_){v.x, v.y, v.z, v.w}) : "memory");
}
__device__ __forceinline__ void st8(void* p, uint2 v) {
  asm volatile("global_store_dwordx2 %0, %1, off sc1\n\ts_nop 1" ::"v"(p), "v"((v2u_){v.x, v.y}) : "memory");
}
__device__ __forceinline__ float bf2f(u16 v) { return __uint_as_float((u32)v << 16); }
__device__ __forceinline__ u16 f2bf(float f) { __bf16 b = (__bf16)f; return __builtin_bit_cast(u16, b); }
__device__ __forceinline__ float rbf(float f) { return (float)(__bf16)f; }
__device__ __forceinline__ float lo16f(u32 v) { return __uint_as_float(v << 16); }
__device__ __forceinline__ float hi16f(u32 v) { return __uint_as_float(v & 0xffff0000u); }
__device__ __forceinline__ u32 pack2(float a, float b) { return (u32)f2bf(a) | ((u32)f2bf(b) << 16); }

__device__ __forceinline__ float fdiv(float a, float b, float r) {
  float q = a * r;
  float e = fmaf(-q, b, a);
  return fmaf(e, r, q);
}
__device__ __forceinline__ float wave_sum(float v) {
#pragma unroll
  for (int o = 32; o > 0; o >>= 1) v += __shfl_xor(v, o);
  return v;
}

typedef float f2 __attribute__((ext_vector_type(2)));
typedef __bf16 bf2 __attribute__((ext_vector_type(2)));
__device__ __forceinline__ f2 cmul(f2 a, f2 b) {
  f2 t = a.xx * b;
  f2 bs = {-b.y, b.x};
  return __builtin_elementwise_fma(a.yy, bs, t);
}
__device__ __forceinline__ f2 rbf2(f2 v) { return __builtin_convertvector(__builtin_convertvector(v, bf2), f2); }
__device__ __forceinline__ u32 sqrelu2(f2 a, f2 b) {
  f2 v = rbf2(rbf2(a) + b);
  f2 z = {0.f, 0.f};
  v = __builtin_elementwise_max(v, z);
  v = v * v;
  return __builtin_bit_cast(u32, __builtin_convertvector(v, bf2));
}
__device__ __forceinline__ f2 cexp_(float are, float aim) {
  float mag = __builtin_amdgcn_exp2f(are * 1.44269504088896340736f);
  const float I2P_HI = 0.15915493667125702f;
  const float I2P_LO = 6.4206383e-09f;
  float t = aim * I2P_HI;
  float e = fmaf(aim, I2P_HI, -t) + aim * I2P_LO;
  float f = (t - rintf(t)) + e;
  f2 r;
  r.x = mag * __builtin_amdgcn_cosf(f);
  r.y = mag * __builtin_amdgcn_sinf(f);
  return r;
}

#define SB() __builtin_amdgcn_sched_barrier(0)
__device__ __forceinline__ constexpr int ekey(int r) {
  return (int)((r < 16 ? 0x3607495fa3d0afd9ull >> (4 * r) : (r < 32 ? 0x32c4fc79db2a54d0ull >> (4 * (r - 16)) : 0x00000000e72ca570ull >> (4 * (r - 32)))) & 15ull);
}
__device__ __forceinline__ constexpr int gkey(int r) {
  return (int)((r < 16 ? 0x581b76be135f9cd3ull >> (4 * r) : (r < 32 ? 0x52fb08d7941be27dull >> (4 * (r - 16)) : 0x00000000906fb42cull >> (4 * (r - 32)))) & 15ull);
}
__device__ __forceinline__ void kg_store(char* tabn, int swb, int idx, int key, f2 v) {
  bf2 h = __builtin_convertvector(v, bf2);
  f2 hf = __builtin_convertvector(h, f2);
  bf2 l = __builtin_convertvector(v - hf, bf2);
  uint2 w = make_uint2(__builtin_bit_cast(u32, h), __builtin_bit_cast(u32, l));
  *(uint2*)(tabn + idx * 1024 + (swb ^ (key << 4))) = w;
}

#define EK_LO0 0x3607495fa3d0afd9ull
#define EK_LO1 0x000054d0u
#define EK_HI0 0xa57032c4fc79db2aull
#define EK_HI1 0x0000e72cu
#define GK_LO0 0x581b76be135f9cd3ull
#define GK_LO1 0x0000e27du
#define GK_HI0 0xb42c52fb08d7941bull
#define GK_HI1 0x0000906fu
__device__ __forceinline__ void kgen_role(int d, const u16* __restrict__ logA, const u16* __restrict__ Aim,
                                          const u16* __restrict__ Cre, const u16* __restrict__ Cim,
                                          const u16* __restrict__ logdt, u16* __restrict__ Kp,
                                          const u16* __restrict__ w1, const u16* __restrict__ w2,
                                          u16* __restrict__ w1T, u16* __restrict__ w2T, char* smem) {
  const int tid = threadIdx.x;
  const bool has_tr = d < 196;
  const u16* tr_in = w2;
  u16* tr_out = w2T;
  const int trR = FF, trC = DD;
  const int tr_r = d / 7, tr_c = d % 7;
  uint4 tr_raw = make_uint4(0, 0, 0, 0);
  if (has_tr) tr_raw = *(const uint4*)(tr_in + (size_t)(tr_r * 64 + (tid >> 3)) * trC + tr_c * 64 + (tid & 7) * 8);
  const int wave = tid >> 6, lane = tid & 63;
  const int role = __builtin_amdgcn_readfirstlane(tid >> 7);
  char* tabE = smem;
  char* tabG = smem + 40 * 1024;
  const int nl = tid & 127;
  STAMP(2, 0);
  u16 in_la[2], in_ai[2], in_cr[2], in_ci[2];
  const u16 in_dt = logdt[d];
#pragma unroll
  for (int pass = 0; pass < 2; ++pass) {
    in_la[pass] = logA[d * NN + pass * 128 + nl];
    in_ai[pass] = Aim[d * NN + pass * 128 + nl];
    in_cr[pass] = Cre[d * NN + pass * 128 + nl];
    in_ci[pass] = Cim[d * NN + pass * 128 + nl];
  }

  const float dt = rbf(expf(bf2f(in_dt)));
  STAMPV(2, 1, dt);
  f32x4 acc = {0.f, 0.f, 0.f, 0.f};
  float last = 0.f;
  const int seg = wave & 3, kh = wave >> 2;
  const int fi = lane & 15, fq = lane >> 4;
  int gidx, eidx;
  if (seg == 0) { gidx = fi; eidx = fi; }
  else {
    gidx = 8 + 8 * seg + (fi & 7);
    int e = fi << seg;
    eidx = e < 16 ? e : (e < 32 ? 16 + ((e - 16) >> 1) : (e < 64 ? 24 + ((e - 32) >> 2) : 32 + ((e - 64) >> 3)));
  }
  const int gk = gkey(gidx), ek = ekey(eidx);
  const int swb = ((nl >> 1) << 4) + ((nl & 1) << 3);

#pragma unroll 1
  for (int pass = 0; pass < 2; ++pass) {
    {
      const u16 p_la = pass ? in_la[1] : in_la[0], p_ai = pass ? in_ai[1] : in_ai[0];
      const u16 p_cr = pass ? in_cr[1] : in_cr[0], p_ci = pass ? in_ci[1] : in_ci[0];
      float ea = rbf(expf(bf2f(p_la)));
      if (role < 2) {
        const float are = -ea * dt;
        const float aim = bf2f(p_ai) * dt;
#define PUT(IDX, V) kg_store(tabE, swb, IDX, ekey(IDX), V);
        f2 b2 = cexp_(are * 2.f, aim * 2.f), b4 = cexp_(are * 4.f, aim * 4.f);
        f2 b8 = cexp_(are * 8.f, aim * 8.f), b16 = cexp_(are * 16.f, aim * 16.f);
        f2 b6 = cmul(b4, b2);
        if (role == 0) {
          f2 b0 = {1.f, 0.f};
          f2 b1 = cexp_(are, aim);
          f2 b3 = cmul(b2, b1), b5 = cmul(b4, b1), b7 = cmul(b4, b3);
          PUT(0, b0) PUT(1, b1) PUT(2, b2) PUT(3, b3) PUT(4, b4) PUT(5, b5) PUT(6, b6) PUT(7, b7)
          PUT(8, b8) PUT(9, cmul(b8, b1)) PUT(10, cmul(b8, b2)) PUT(11, cmul(b8, b3)) PUT(12, cmul(b8, b4))
          PUT(13, cmul(b8, b5)) PUT(14, cmul(b8, b6)) PUT(15, cmul(b8, b7))
          PUT(16, b16) PUT(17, cmul(b16, b2)) PUT(18, cmul(b16, b4)) PUT(19, cmul(b16, b6))
        } else {
          f2 b32 = cexp_(are * 32.f, aim * 32.f), b64 = cexp_(are * 64.f, aim * 64.f);
          f2 b10 = cmul(b8, b2), b12 = cmul(b8, b4), b14 = cmul(b8, b6);
          f2 b20 = cmul(b16, b4), b24 = cmul(b16, b8), b28 = cmul(b16, b12);
          PUT(20, b24) PUT(21, cmul(b16, b10)) PUT(22, b28) PUT(23, cmul(b16, b14))
          f2 b40 = cmul(b32, b8), b48 = cmul(b32, b16), b56 = cmul(b32, b24);
          PUT(24, b32) PUT(25, cmul(b32, b4)) PUT(26, b40) PUT(27, cmul(b32, b12)) PUT(28, b48)
          PUT(29, cmul(b32, b20)) PUT(30, b56) PUT(31, cmul(b32, b28))
          PUT(32, b64) PUT(33, cmul(b64, b8)) PUT(34, cmul(b64, b16)) PUT(35, cmul(b64, b24)) PUT(36, cmul(b64, b32))
          PUT(37, cmul(b64, b40)) PUT(38, cmul(b64, b48)) PUT(39, cmul(b64, b56))
        }
#undef PUT
      } else {
        const float are = (-ea * dt) * 16.f;
        const float aim = -(bf2f(p_ai) * dt) * 16.f;
        f2 c;
        c.x = bf2f(p_cr) * dt;
        c.y = -(bf2f(p_ci) * dt);
#define PUT(IDX, V) kg_store(tabG, swb, IDX, gkey(IDX), V);
        f2 b2 = cexp_(are * 2.f, aim * 2.f), b4 = cexp_(are * 4.f, aim * 4.f);
        f2 b8 = cexp_(are * 8.f, aim * 8.f), b16 = cexp_(are * 16.f, aim * 16.f);
        f2 b6 = cmul(b4, b2);
        f2 cb16 = cmul(c, b16);
        if (role == 2) {
          f2 b1 = cexp_(are, aim);
          f2 b3 = cmul(b2, b1), b5 = cmul(b4, b1), b7 = cmul(b4, b3);
          f2 cb8 = cmul(c, b8);
          PUT(0, c) PUT(1, cmul(c, b1)) PUT(2, cmul(c, b2)) PUT(3, cmul(c, b3)) PUT(4, cmul(c, b4))
          PUT(5, cmul(c, b5)) PUT(6, cmul(c, b6)) PUT(7, cmul(c, b7))
          PUT(8, cb8) PUT(9, cmul(cb8, b1)) PUT(10, cmul(cb8, b2)) PUT(11, cmul(cb8, b3)) PUT(12, cmul(cb8, b4))
          PUT(13, cmul(cb8, b5)) PUT(14, cmul(cb8, b6)) PUT(15, cmul(cb8, b7))
          PUT(16, cb16) PUT(17, cmul(cb16, b2)) PUT(18, cmul(cb16, b4)) PUT(19, cmul(cb16, b6))
        } else {
          f2 b32 = cexp_(are * 32.f, aim * 32.f), b64 = cexp_(are * 64.f, aim * 64.f);
          f2 b128 = cexp_(are * 128.f, aim * 128.f);
          last += cmul(c, b128).x;
          f2 cb32 = cmul(c, b32), cb64 = cmul(c, b64);
          f2 b10 = cmul(b8, b2), b12 = cmul(b8, b4), b14 = cmul(b8, b6);
          PUT(20, cmul(cb16, b8)) PUT(21, cmul(cb16, b10)) PUT(22, cmul(cb16, b12)) PUT(23, cmul(cb16, b14))
          f2 b20 = cmul(b16, b4), b24 = cmul(b16, b8), b28 = cmul(b16, b12);
          PUT(24, cb32) PUT(25, cmul(cb32, b4)) PUT(26, cmul(cb32, b8)) PUT(27, cmul(cb32, b12)) PUT(28, cmul(cb32, b16))
          PUT(29, cmul(cb32, b20)) PUT(30, cmul(cb32, b24)) PUT(31, cmul(cb32, b28))
          f2 b40 = cmul(b32, b8), b48 = cmul(b32, b16), b56 = cmul(b32, b24);
          PUT(32, cb64) PUT(33, cmul(cb64, b8)) PUT(34, cmul(cb64, b16)) PUT(35, cmul(cb64, b24)) PUT(36, cmul(cb64, b32))
          PUT(37, cmul(cb64, b40)) PUT(38, cmul(cb64, b48)) PUT(39, cmul(cb64, b56))
        }
#undef PUT
      }
    }
    STAMP(2, 2 + pass * 3);
    __syncthreads();
    STAMP(2, 3 + pass * 3);
    if (pass == 0) STAMP(1, 3);
#pragma unroll
    for (int kb = 0; kb < 2; ++kb) {
      uint4 g[4], e[4];
#pragma unroll
      for (int ks = 0; ks < 4; ++ks) {
        int chunk = (kh * 8 + kb * 4 + ks) * 4 + fq;
        g[ks] = *(const uint4*)(tabG + gidx * 1024 + ((chunk ^ gk) << 4));
        e[ks] = *(const uint4*)(tabE + eidx * 1024 + ((chunk ^ ek) << 4));
      }
      SB();
#pragma unroll
      for (int ks = 0; ks < 4; ++ks) {
        uint4 es = make_uint4(e[ks].y, e[ks].x, e[ks].w, e[ks].z);
        acc = __builtin_amdgcn_mfma_f32_16x16x32_bf16(__builtin_bit_cast(bf16x8, g[ks]), __builtin_bit_cast(bf16x8, es), acc, 0, 0, 0);
        acc = __builtin_amdgcn_mfma_f32_16x16x32_bf16(__builtin_bit_cast(bf16x8, g[ks]), __builtin_bit_cast(bf16x8, e[ks]), acc, 0, 0, 0);
      }
      SB();
    }
    __syncthreads();
    STAMP(2, 4 + pass * 3);
  }
  float* Kd = (float*)(smem + 74752);
  float* red = Kd + 1296;
  last = wave_sum(last);
  if (lane == 0) red[wave] = last;
#pragma unroll
  for (int r = 0; r < 4; ++r) {
    int row = fq * 4 + r;
    if (seg == 0) Kd[kh * 648 + row * 16 + fi] = acc[r];
    else if (row < 8) Kd[kh * 648 + 256 + (seg - 1) * 128 + row * 16 + fi] = acc[r];
  }
  __syncthreads();
  {
    const int g = tid >> 1, hf = tid & 1;
    u32 wh[2], wl[2];
#pragma unroll
    for (int jj = 0; jj < 2; ++jj) {
      f2 kv;
#pragma unroll
      for (int e = 0; e < 2; ++e) {
        int l = 8 * g + 7 - (4 * hf + 2 * jj + e);
        int p = (int)rbf((float)l);
        int idx;
        if (p < 256) idx = p;
        else {
          int s = (31 - __clz(p)) - 7;
          idx = 256 + (s - 1) * 128 + ((p >> s) - 128);
        }
        int ix = idx < 640 ? idx : 0;
        float v = Kd[ix] + Kd[648 + ix];
        if (idx >= 640) v = red[6] + red[7];
        kv[e] = 2.f * v;
      }
      bf2 hq = __builtin_convertvector(kv, bf2);
      bf2 lq = __builtin_convertvector(kv - __builtin_convertvector(hq, f2), bf2);
      wh[jj] = __builtin_bit_cast(u32, hq);
      wl[jj] = __builtin_bit_cast(u32, lq);
    }
    char* ko = (char*)(Kp + (size_t)d * 2 * KPAD);
    st8(ko + 512 + 8 * tid, make_uint2(wh[0], wh[1]));
    st8(ko + 4608 + 512 + 8 * tid, make_uint2(wl[0], wl[1]));
    if (tid < 32) {
      *(uint4*)(ko + 16 * tid) = make_uint4(0, 0, 0, 0);
      *(uint4*)(ko + 4608 + 16 * tid) = make_uint4(0, 0, 0, 0);
    }
  }
  STAMP(2, 8);
  if (has_tr) {
    u16* tile = (u16*)smem;
    u32* tw = (u32*)(tile + (tid >> 3) * 66 + (tid & 7) * 8);
    tw[0] = tr_raw.x; tw[1] = tr_raw.y; tw[2] = tr_raw.z; tw[3] = tr_raw.w;
    __syncthreads();
    const int c = tid >> 3, r8 = tid & 7;
    u32 w[4];
#pragma unroll
    for (int j = 0; j < 4; ++j)
      w[j] = (u32)tile[(r8 * 8 + 2 * j) * 66 + c] | ((u32)tile[(r8 * 8 + 2 * j + 1) * 66 + c] << 16);
    st16(tr_out + (size_t)(tr_c * 64 + c) * trR + tr_r * 64 + r8 * 8, make_uint4(w[0], w[1], w[2], w[3]));
  }
}

__device__ void ln_role(int blk, const u16* __restrict__ x, u16* __restrict__ xn, u16* __restrict__ xnT, char* smem) {
  const int tid = threadIdx.x, wave = tid >> 6, lane = tid & 63;
  u16* tile = (u16*)smem;
  const int row = wave * 8 + (lane >> 3), c = lane & 7;
  const int l = blk * 64 + row;
  STAMP(0, 0);
  uint4 raw[7];
#pragma unroll
  for (int j = 0; j < 7; ++j) raw[j] = *(const uint4*)(x + (size_t)l * DD + j * 64 + c * 8);
  float s = 0.f;
#pragma unroll
  for (int j = 0; j < 7; ++j) {
    s += (lo16f(raw[j].x) + hi16f(raw[j].x)) + (lo16f(raw[j].y) + hi16f(raw[j].y));
    s += (lo16f(raw[j].z) + hi16f(raw[j].z)) + (lo16f(raw[j].w) + hi16f(raw[j].w));
  }
  s += __shfl_xor(s, 1); s += __shfl_xor(s, 2); s += __shfl_xor(s, 4);
  const float mean = s * (1.f / DD);
  STAMP(0, 1);
  float q = 0.f;
#pragma unroll
  for (int j = 0; j < 7; ++j) {
    u32 w4[4] = {raw[j].x, raw[j].y, raw[j].z, raw[j].w};
#pragma unroll
    for (int e = 0; e < 4; ++e) {
      float t0 = lo16f(w4[e]) - mean, t1 = hi16f(w4[e]) - mean;
      q = fmaf(t0, t0, q); q = fmaf(t1, t1, q);
    }
  }
  q += __shfl_xor(q, 1); q += __shfl_xor(q, 2); q += __shfl_xor(q, 4);
  const float var = q * (1.f / DD);
  const float mb = rbf(mean), vb = rbf(var);
  const float den = rbf(sqrtf(rbf(vb + 1e-5f)));
  const float rden = 1.0f / den;
#pragma unroll
  for (int j = 0; j < 7; ++j) {
    u32 w4[4] = {raw[j].x, raw[j].y, raw[j].z, raw[j].w};
    u32 o[4];
#pragma unroll
    for (int e = 0; e < 4; ++e)
      o[e] = pack2(fdiv(rbf(lo16f(w4[e]) - mb), den, rden), fdiv(rbf(hi16f(w4[e]) - mb), den, rden));
    u32* trow = (u32*)(tile + row * 450 + j * 64 + c * 8);
    trow[0] = o[0]; trow[1] = o[1]; trow[2] = o[2]; trow[3] = o[3];
  }
  STAMP(0, 2);
  __syncthreads();
  STAMP(0, 3);
  for (int v = tid; v < 2 * DD; v += 512) {
    const int d = v >> 1, rh = (v & 1) * 32;
    u32 w[16];
#pragma unroll
    for (int r = 0; r < 16; ++r)
      w[r] = (u32)tile[(rh + 2 * r) * 450 + d] | ((u32)tile[(rh + 2 * r + 1) * 450 + d] << 16);
    uint4* dst = (uint4*)(xnT + (size_t)d * LL + blk * 64 + rh);
    st16(dst, make_uint4(w[0], w[1], w[2], w[3]));
    st16(dst + 1, make_uint4(w[4], w[5], w[6], w[7]));
    st16(dst + 2, make_uint4(w[8], w[9], w[10], w[11]));
    st16(dst + 3, make_uint4(w[12], w[13], w[14], w[15]));
  }
  STAMP(0, 4);
}

__device__ void tr_role(const u16* __restrict__ in, u16* __restrict__ out, int R, int C, int tr, int tc, char* smem) {
  const int tid = threadIdx.x;
  u16* tile = (u16*)smem;
  __syncthreads();
  {
    int v = tid;
    int r = v >> 3, c8 = v & 7;
    uint4 raw = *(const uint4*)(in + (size_t)(tr * 64 + r) * C + tc * 64 + c8 * 8);
    u32* t = (u32*)(tile + r * 66 + c8 * 8);
    t[0] = raw.x; t[1] = raw.y; t[2] = raw.z; t[3] = raw.w;
  }
  __syncthreads();
  {
    int v = tid;
    int c = v >> 3, r8 = v & 7;
    u32 w[4];
#pragma unroll
    for (int j = 0; j < 4; ++j)
      w[j] = (u32)tile[(r8 * 8 + 2 * j) * 66 + c] | ((u32)tile[(r8 * 8 + 2 * j + 1) * 66 + c] << 16);
    *(uint4*)(out + (size_t)(tc * 64 + c) * R + tr * 64 + r8 * 8) = make_uint4(w[0], w[1], w[2], w[3]);
  }
}

typedef __attribute__((ext_vector_type(4))) short s16x4;
typedef __attribute__((address_space(3))) s16x4* lds_s4p;
__device__ __forceinline__ void g1_role(int blk, const u16* __restrict__ x, const u16* __restrict__ w1,
                                        const u16* __restrict__ b1, u16* __restrict__ h, char* smem) {
  const int tid = threadIdx.x, wave = tid >> 6, lane = tid & 63;
  const int xcd = blk & 7, idx = blk >> 3;
  const int mt = xcd * 2 + (idx & 1), nt = idx >> 1;
  const int l0 = mt * 128, f0 = nt * 128;
  const int wr = wave >> 2, wc = wave & 3;
  const int r32 = lane & 31, hh = lane >> 5;
  const int xrow = wave * 8 + (lane >> 3), xc = lane & 7;
  uint4 raw[2][7];
#pragma unroll
  for (int hf = 0; hf < 2; ++hf)
#pragma unroll
    for (int j = 0; j < 7; ++j)
      raw[hf][j] = *(const uint4*)(x + (size_t)(l0 + hf * 64 + xrow) * DD + j * 64 + xc * 8);
  const u16* srcW[2];
#pragma unroll
  for (int i = 0; i < 2; ++i) {
    int row = (wave * 2 + i) * 4 + (lane >> 4);
    int src = (lane & 15) ^ (((row & 3) << 2) | ((row >> 2) & 3));
    srcW[i] = w1 + (size_t)row * FF + f0 + src * 8;
  }
  u32 woff = 0;
  auto gl = [&](int t) {
#pragma unroll
    for (int i = 0; i < 2; ++i)
      __builtin_amdgcn_global_load_lds((const u32*)((const char*)srcW[i] + woff),
                                       (u32*)(smem + (t % 3) * 16384 + (wave * 2 + i) * 1024 + lane * 16), 16, 0, 0);
    woff += 64 * FF * 2;
    asm volatile("" : "+s"(woff));
  };
  gl(0);
  gl(1);
#pragma unroll
  for (int hf = 0; hf < 2; ++hf) {
    float sm = 0.f;
#pragma unroll
    for (int j = 0; j < 7; ++j) {
      sm += (lo16f(raw[hf][j].x) + hi16f(raw[hf][j].x)) + (lo16f(raw[hf][j].y) + hi16f(raw[hf][j].y));
      sm += (lo16f(raw[hf][j].z) + hi16f(raw[hf][j].z)) + (lo16f(raw[hf][j].w) + hi16f(raw[hf][j].w));
    }
    sm += __shfl_xor(sm, 1); sm += __shfl_xor(sm, 2); sm += __shfl_xor(sm, 4);
    const float mean = sm * (1.f / DD);
#pragma unroll
    for (int j = 0; j < 7; ++j)
      asm volatile("" : "+v"(raw[hf][j].x), "+v"(raw[hf][j].y), "+v"(raw[hf][j].z), "+v"(raw[hf][j].w));
    float q = 0.f;
#pragma unroll
    for (int j = 0; j < 7; ++j) {
      u32 w4[4] = {raw[hf][j].x, raw[hf][j].y, raw[hf][j].z, raw[hf][j].w};
#pragma unroll
      for (int e = 0; e < 4; ++e) {
        float t0 = lo16f(w4[e]) - mean, t1 = hi16f(w4[e]) - mean;
        q = fmaf(t0, t0, q); q = fmaf(t1, t1, q);
      }
      SB();
    }
    q += __shfl_xor(q, 1); q += __shfl_xor(q, 2); q += __shfl_xor(q, 4);
#pragma unroll
    for (int j = 0; j < 7; ++j)
      asm volatile("" : "+v"(raw[hf][j].x), "+v"(raw[hf][j].y), "+v"(raw[hf][j].z), "+v"(raw[hf][j].w));
    const float mb = rbf(mean), vb = rbf(q * (1.f / DD));
    const float den = rbf(sqrtf(rbf(vb + 1e-5f)));
    const float rden = 1.0f / den;
#pragma unroll
    for (int j = 0; j < 7; ++j) {
      u32 w4[4] = {raw[hf][j].x, raw[hf][j].y, raw[hf][j].z, raw[hf][j].w};
#pragma unroll
      for (int e = 0; e < 4; ++e)
        w4[e] = pack2(fdiv(rbf(lo16f(w4[e]) - mb), den, rden), fdiv(rbf(hi16f(w4[e]) - mb), den, rden));
      asm volatile("" : "+v"(w4[0]), "+v"(w4[1]), "+v"(w4[2]), "+v"(w4[3]));
      raw[hf][j] = make_uint4(w4[0], w4[1], w4[2], w4[3]);
      SB();
    }
  }
  char* xs = smem + 49152;
  const int xw0 = xrow * 128 + ((xc ^ ((xrow >> 1) & 7)) << 4);
  const int xw1 = (64 + xrow) * 128 + ((xc ^ ((xrow >> 1) & 7)) << 4);
  *(uint4*)(xs + xw0) = raw[0][0];
  *(uint4*)(xs + xw1) = raw[1][0];
  int tb[2][2];
  const int tq = (lane & 15) >> 2, tp = lane & 3, tblk = (lane >> 4) & 1;
  const int brow = wc * 32 + r32;
  f32x16 acc[2];
#pragma unroll
  for (int i = 0; i < 2; ++i) {
#pragma unroll
    for (int u = 0; u < 2; ++u)
      tb[i][u] = 256 * (8 * hh + 4 * u + tq) +
                 16 * ((4 * (wr * 2 + i) + 2 * tblk + (tp >> 1)) ^ ((tq << 2) | ((2 * hh + u) & 3))) + 8 * (tp & 1);
#pragma unroll
    for (int r = 0; r < 16; ++r) acc[i][r] = 0.f;
  }
#pragma unroll
  for (int t = 0; t < 7; ++t) {
    if (t + 1 < 7) asm volatile("s_waitcnt vmcnt(2) lgkmcnt(0)" ::: "memory");
    else asm volatile("s_waitcnt vmcnt(0) lgkmcnt(0)" ::: "memory");
    __builtin_amdgcn_s_barrier();
    if (t + 2 < 7) gl(t + 2);
    if (t + 1 < 7) {
      *(uint4*)(xs + ((t + 1) & 1) * 16384 + xw0) = raw[0][t + 1];
      *(uint4*)(xs + ((t + 1) & 1) * 16384 + xw1) = raw[1][t + 1];
    }
    const char* ws_ = smem + (t % 3) * 16384;
    const char* xb = xs + (t & 1) * 16384;
#pragma unroll
    for (int kk = 0; kk < 4; ++kk) {
      bf16x8 bq = *(const bf16x8*)(xb + brow * 128 + (((2 * kk + hh) ^ ((brow >> 1) & 7)) << 4));
      bf16x8 a[2];
#pragma unroll
      for (int i = 0; i < 2; ++i) {
        s16x4 v[2];
#pragma unroll
        for (int u = 0; u < 2; ++u) {
          v[u] = __builtin_amdgcn_ds_read_tr16_b64_v4i16((lds_s4p)(ws_ + tb[i][u] + 4096 * kk));
        }
        a[i] = __builtin_shufflevector(v[0], v[1], 0, 1, 2, 3, 4, 5, 6, 7);
      }
      acc[0] = __builtin_amdgcn_mfma_f32_32x32x16_bf16(a[0], bq, acc[0], 0, 0, 0);
      acc[1] = __builtin_amdgcn_mfma_f32_32x32x16_bf16(a[1], bq, acc[1], 0, 0, 0);
      if (kk == 1) SB();
    }
  }
  uint2 braw_[2][4];
#pragma unroll
  for (int i = 0; i < 2; ++i)
#pragma unroll
    for (int g = 0; g < 4; ++g) braw_[i][g] = *(const uint2*)(b1 + f0 + wr * 64 + i * 32 + 8 * g + 4 * hh);
  __syncthreads();
  {
    const int ll = wc * 32 + r32;
#pragma unroll
    for (int i = 0; i < 2; ++i)
#pragma unroll
      for (int g = 0; g < 4; ++g) {
        int fl = wr * 64 + i * 32 + 8 * g + 4 * hh;
        uint2 braw = braw_[i][g];
        f2 bb0 = {lo16f(braw.x), hi16f(braw.x)}, bb1 = {lo16f(braw.y), hi16f(braw.y)};
        f2 a0 = {acc[i][4 * g], acc[i][4 * g + 1]}, a1 = {acc[i][4 * g + 2], acc[i][4 * g + 3]};
        *(uint2*)(smem + ll * 256 + (((fl >> 3) ^ (ll & 15)) << 4) + (fl & 4) * 2) =
            make_uint2(sqrelu2(a0, bb0), sqrelu2(a1, bb1));
      }
  }
  __syncthreads();
#pragma unroll
  for (int i = 0; i < 4; ++i) {
    int row = i * 32 + (tid >> 4), ch = tid & 15;
    uint4 v = *(const uint4*)(smem + row * 256 + ((ch ^ (row & 15)) << 4));
    st16(h + (size_t)(l0 + row) * FF + f0 + ch * 8, v);
  }
}

__global__ __launch_bounds__(512, 4) void k1_prep(const u16* __restrict__ x, const u16* __restrict__ logA,
                                                  const u16* __restrict__ Aim, const u16* __restrict__ Cre,
                                                  const u16* __restrict__ Cim, const u16* __restrict__ logdt,
                                                  const u16* __restrict__ w1, const u16* __restrict__ b1,
                                                  const u16* __restrict__ w2, char* ws) {
  __shared__ __attribute__((aligned(16))) char smem[81920];
  const int b = blockIdx.x;
  RSTAMP(2, 14);
  if (b < 224) {
    g1_role(b, x, w1, b1, (u16*)(ws + WS_H), smem);
  } else if (b < 256) {
    if (b == 224 && threadIdx.x < 32) *(u32*)(ws + WS_CNT + threadIdx.x * 128) = 0u;
    ln_role(b - 224, x, (u16*)(ws + WS_XN), (u16*)(ws + WS_XNT), smem);
  } else {
    kgen_role(b - 256, logA, Aim, Cre, Cim, logdt, (u16*)(ws + WS_KP), w1, w2, (u16*)(ws + WS_W1T),
              (u16*)(ws + WS_W2T), smem);
  }
  RSTAMP(2, 15);
}

#define RLX_AGENT __ATOMIC_RELAXED, __HIP_MEMORY_SCOPE_AGENT
typedef __attribute__((address_space(1))) unsigned gu32;
typedef __attribute__((ext_vector_type(4))) unsigned v4u;

__device__ __forceinline__ void wait_count(u32* word, unsigned want) {
  for (unsigned spins = 0; spins < (1u << 22); ++spins) {
    unsigned v = __builtin_amdgcn_readfirstlane(__hip_atomic_load(word, RLX_AGENT));
    if (v >= want) break;
    __builtin_amdgcn_s_sleep(1);
  }
}

__device__ __forceinline__ void x_role(int blk, char* ws, const u16* __restrict__ b1, const u16* __restrict__ x,
                                       const u16* __restrict__ b2, u16* __restrict__ out, char* smem) {
  const int tid = threadIdx.x, wave = tid >> 6, lane = tid & 63;
  const int xcd = blk & 7, idx = blk >> 3;
  const int r32 = lane & 31, hh = lane >> 5;
  const u16* xn = (const u16*)(ws + WS_XN);
  const u16* w1T = (const u16*)(ws + WS_W1T);
  const u16* w2T = (const u16*)(ws + WS_W2T);
  u16* h = (u16*)(ws + WS_H);
  const u16* sT = (const u16*)(ws + WS_ST);
  u32* cnt = (u32*)(ws + WS_CNT);
  f32x16 acc[2];
  STAMP(1, 0);
  {
    const int mt = xcd * 4 + (idx & 3), nt = idx >> 2;
    const int l0 = mt * 64, d0 = nt * 64;
    const int kq = wave & 3, wi = wave >> 2;
    const u16* srcA;
    const u16* srcB;
    {
      int row = wave * 8 + (lane >> 3);
      int src = (lane & 7) ^ ((row >> 1) & 7);
      srcA = w2T + (size_t)(d0 + row) * FF + src * 8;
      srcB = h + (size_t)(l0 + row) * FF + src * 8;
    }
    auto gls = [&](int t, int slot, int q) {
      char* dst = smem + slot * 16384 + q * 8192 + wave * 1024 + lane * 16;
      const u16* sp = (q ? srcB : srcA) + t * 64;
      __builtin_amdgcn_global_load_lds((const u32*)sp, (u32*)dst, 16, 0, 0);
    };
    auto gl = [&](int t, int q) { gls(t, t % 5, q); };
    const int quad = wave >> 1, half = wave & 1;
    const int er = quad >> 1, ec = quad & 1;
    const int l = l0 + ec * 32 + r32;
    uint2 braw_[2], xraw_[2];
#pragma unroll
    for (int g2 = 0; g2 < 2; ++g2) {
      int d = d0 + er * 32 + 8 * (half * 2 + g2) + 4 * hh;
      braw_[g2] = *(const uint2*)(b2 + d);
      xraw_[g2] = *(const uint2*)(x + (size_t)l * DD + d);
    }
#pragma unroll
    for (int t = 0; t < 4; ++t) gl(t, 0);
#pragma unroll
    for (int i = 0; i < 2; ++i)
#pragma unroll
      for (int r = 0; r < 16; ++r) acc[i][r] = 0.f;
#pragma unroll
    for (int t = 0; t < 4; ++t) gl(t, 1);
    int fo[3];
#pragma unroll
    for (int w = 0; w < 3; ++w) {
      int row = (w == 0) ? (wi * 32 + r32) : ((w - 1) * 32 + r32);
      int ch = 2 * kq + hh;
      fo[w] = (w ? 8192 : 0) + row * 128 + ((ch ^ ((row >> 1) & 7)) << 4);
    }
    auto step2 = [&](int t, int slot, int slot4, bool issue) {
      const char* bs = smem + slot * 16384;
      bf16x8 fr[3];
#pragma unroll
      for (int w = 0; w < 3; ++w) fr[w] = *(const bf16x8*)(bs + fo[w]);
      SB();
      if (issue) gls(t + 4, slot4, 0);
      acc[0] = __builtin_amdgcn_mfma_f32_32x32x16_bf16(fr[0], fr[1], acc[0], 0, 0, 0);
      SB();
      if (issue) gls(t + 4, slot4, 1);
      acc[1] = __builtin_amdgcn_mfma_f32_32x32x16_bf16(fr[0], fr[2], acc[1], 0, 0, 0);
      SB();
    };
    {
      asm volatile("s_waitcnt vmcnt(3) lgkmcnt(0)" ::: "memory");
      __builtin_amdgcn_s_barrier();
      step2(0, 0, 4, true);
      asm volatile("s_waitcnt vmcnt(4) lgkmcnt(0)" ::: "memory");
      __builtin_amdgcn_s_barrier();
      step2(1, 1, 0, true);
      asm volatile("s_waitcnt vmcnt(5) lgkmcnt(0)" ::: "memory");
      __builtin_amdgcn_s_barrier();
      step2(2, 2, 1, true);
      int slot = 3;
#pragma unroll 1
      for (int t = 3; t < 24; ++t) {
        asm volatile("s_waitcnt vmcnt(6) lgkmcnt(0)" ::: "memory");
        __builtin_amdgcn_s_barrier();
        step2(t, slot, slot == 0 ? 4 : slot - 1, true);
        slot = slot == 4 ? 0 : slot + 1;
      }
      asm volatile("s_waitcnt vmcnt(6) lgkmcnt(0)" ::: "memory");
      __builtin_amdgcn_s_barrier();
      step2(24, 4, 0, false);
      asm volatile("s_waitcnt vmcnt(4) lgkmcnt(0)" ::: "memory");
      __builtin_amdgcn_s_barrier();
      step2(25, 0, 0, false);
      asm volatile("s_waitcnt vmcnt(2) lgkmcnt(0)" ::: "memory");
      __builtin_amdgcn_s_barrier();
      step2(26, 1, 0, false);
      asm volatile("s_waitcnt vmcnt(0) lgkmcnt(0)" ::: "memory");
      __builtin_amdgcn_s_barrier();
      step2(27, 2, 0, false);
    }
    STAMP(1, 4);
    __syncthreads();
    float* red = (float*)smem;
#pragma unroll
    for (int j = 0; j < 2; ++j)
#pragma unroll
      for (int r4 = 0; r4 < 4; ++r4)
        *(float4*)(red + ((kq * 4 + wi * 2 + j) * 4 + r4) * 256 + lane * 4) =
            make_float4(acc[j][4 * r4], acc[j][4 * r4 + 1], acc[j][4 * r4 + 2], acc[j][4 * r4 + 3]);
    if (wave == 0) wait_count(cnt + (16 + nt) * 32, 64u);
    __syncthreads();
    STAMP(1, 5);
    u16 sraw_[8];
#pragma unroll
    for (int g2 = 0; g2 < 2; ++g2) {
      int d = d0 + er * 32 + 8 * (half * 2 + g2) + 4 * hh;
#pragma unroll
      for (int e = 0; e < 4; ++e) sraw_[4 * g2 + e] = __hip_atomic_load(sT + (size_t)(d + e) * LL + l, RLX_AGENT);
    }
    float fin[8];
#pragma unroll
    for (int r = 0; r < 8; ++r) fin[r] = 0.f;
#pragma unroll
    for (int w = 0; w < 4; ++w)
#pragma unroll
      for (int g2 = 0; g2 < 2; ++g2) {
        float4 v = *(const float4*)(red + ((w * 4 + quad) * 4 + half * 2 + g2) * 256 + lane * 4);
        fin[4 * g2] += v.x; fin[4 * g2 + 1] += v.y; fin[4 * g2 + 2] += v.z; fin[4 * g2 + 3] += v.w;
      }
#pragma unroll
    for (int g2 = 0; g2 < 2; ++g2) {
      int d = d0 + er * 32 + 8 * (half * 2 + g2) + 4 * hh;
      uint2 braw = braw_[g2];
      uint2 xraw = xraw_[g2];
      float bb[4] = {lo16f(braw.x), hi16f(braw.x), lo16f(braw.y), hi16f(braw.y)};
      float xx[4] = {lo16f(xraw.x), hi16f(xraw.x), lo16f(xraw.y), hi16f(xraw.y)};
      float o[4];
#pragma unroll
      for (int e = 0; e < 4; ++e) {
        float v = rbf(rbf(fin[4 * g2 + e]) + bb[e]);
        float gg = rbf(v * __builtin_amdgcn_rcpf(1.f + __builtin_amdgcn_exp2f(v * -1.44269504088896340736f)));
        float s = bf2f(sraw_[4 * g2 + e]);
        o[e] = xx[e] + rbf(s * gg);
      }
      *(uint2*)(out + (size_t)l * DD + d) = make_uint2(pack2(o[0], o[1]), pack2(o[2], o[3]));
    }
    STAMP(1, 6);
  }
}

#define CV_KOFF 0
#define CV_WOFF 9216
#define CV_PHIP 5136
#define CV_ZOFF (CV_WOFF + 8 * CV_PHIP)
#define CV_KD 74752
__device__ __forceinline__ void conv_one(int d, uint4 k0, uint4 k1, uint4 cur, uint4 prv, u16 dkraw, bool drain,
                                         u16* __restrict__ sT, char* smem) {
  int tid = threadIdx.x;
  asm volatile("" : "+v"(tid));
  const int wave = tid >> 6, lane = tid & 63;
  const int t2 = tid & 255;
  STAMP(1, 0);
  if (t2 == 0) prv = make_uint4(0, 0, 0, 0);
  {
    uint4* dst = (uint4*)(smem + CV_KOFF);
    dst[tid] = k0;
    if (tid < 64) dst[tid + 512] = k1;
  }
  if (tid >= 256) {
    u32 Dw[8] = {prv.x, prv.y, prv.z, prv.w, cur.x, cur.y, cur.z, cur.w};
    char* wb = smem + CV_WOFF + (t2 >> 5) * 640 + (t2 & 31) * 16;
#pragma unroll
    for (int phi = 0; phi < 8; ++phi) {
      int e = phi + 1;
      int k = e >> 1;
      uint4 o;
      if ((e & 1) == 0) {
        o = make_uint4(Dw[k], Dw[k + 1], Dw[k + 2], Dw[k + 3]);
      } else {
        o.x = __builtin_amdgcn_alignbit(Dw[k + 1], Dw[k], 16);
        o.y = __builtin_amdgcn_alignbit(Dw[k + 2], Dw[k + 1], 16);
        o.z = __builtin_amdgcn_alignbit(Dw[k + 3], Dw[k + 2], 16);
        o.w = __builtin_amdgcn_alignbit(Dw[k + 4], Dw[k + 3], 16);
      }
      *(uint4*)(wb + phi * CV_PHIP) = o;
    }
    if (t2 == 0) *(uint4*)(smem + CV_ZOFF) = make_uint4(0, 0, 0, 0);
  }
  __syncthreads();
  STAMP(1, 1);
  const int r32 = lane & 31, hh = lane >> 5;
  const int phi = r32 & 7, al = r32 >> 3;
  f32x16 acc0, acc1;
#pragma unroll
  for (int r = 0; r < 16; ++r) { acc0[r] = 0.f; acc1[r] = 0.f; }
  {
    bf16x8 fr[2][6];
    auto rd = [&](int g, int w) -> bf16x8 {
      int dl = g >> 1, tt = g & 1;
      int v = 2 * (wave * 2 + tt) + hh;
      int off;
      if (w < 4) {
        off = CV_KOFF + 512 + 512 * dl + 16 * (r32 - v) + (w & 1) * 4608 + (w >> 1) * 2048;
      } else {
        int ci = al - dl + (w - 4) * 4;
        off = ci < 0 ? CV_ZOFF : (CV_WOFF + phi * CV_PHIP + ci * 640 + v * 16);
      }
      return *(const bf16x8*)(smem + off);
    };
#pragma unroll
    for (int w = 0; w < 6; ++w) fr[0][w] = rd(0, w);
    SB();
#pragma unroll
    for (int g = 0; g < 8; ++g) {
      const int c = g & 1, n = c ^ 1;
      acc0 = __builtin_amdgcn_mfma_f32_32x32x16_bf16(fr[c][1], fr[c][4], acc0, 0, 0, 0);
      if (g < 7) fr[n][4] = rd(g + 1, 4);
      SB();
      acc1 = __builtin_amdgcn_mfma_f32_32x32x16_bf16(fr[c][3], fr[c][4], acc1, 0, 0, 0);
      if (g < 7) fr[n][1] = rd(g + 1, 1);
      SB();
      acc0 = __builtin_amdgcn_mfma_f32_32x32x16_bf16(fr[c][0], fr[c][4], acc0, 0, 0, 0);
      if (g < 7) fr[n][3] = rd(g + 1, 3);
      SB();
      acc1 = __builtin_amdgcn_mfma_f32_32x32x16_bf16(fr[c][2], fr[c][4], acc1, 0, 0, 0);
      if (g < 7) fr[n][0] = rd(g + 1, 0);
      SB();
      acc1 = __builtin_amdgcn_mfma_f32_32x32x16_bf16(fr[c][1], fr[c][5], acc1, 0, 0, 0);
      if (g < 7) fr[n][2] = rd(g + 1, 2);
      SB();
      acc1 = __builtin_amdgcn_mfma_f32_32x32x16_bf16(fr[c][0], fr[c][5], acc1, 0, 0, 0);
      if (g < 7) fr[n][5] = rd(g + 1, 5);
      SB();
    }
  }
  STAMP(1, 2);
  __syncthreads();
  float* Y = (float*)(smem + CV_WOFF);
#pragma unroll
  for (int reg = 0; reg < 16; ++reg) {
    int r = (reg & 3) + 8 * (reg >> 2) + 4 * hh;
    int l = 8 * r + phi + 256 * al;
    Y[wave * 2048 + l] = acc0[reg];
    Y[wave * 2048 + l + 1024] = acc1[reg];
  }
  __syncthreads();
  if (tid >= 256) {
    float y8[8];
#pragma unroll
    for (int j = 0; j < 8; ++j) y8[j] = 0.f;
#pragma unroll
    for (int w = 0; w < 8; ++w) {
      const float4* p = (const float4*)(Y + w * 2048 + 8 * t2);
      float4 a = p[0], b = p[1];
      y8[0] += a.x; y8[1] += a.y; y8[2] += a.z; y8[3] += a.w;
      y8[4] += b.x; y8[5] += b.y; y8[6] += b.z; y8[7] += b.w;
    }
    float dk = bf2f(dkraw);
    u32 cw[4] = {cur.x, cur.y, cur.z, cur.w};
    u32 o[4];
#pragma unroll
    for (int j = 0; j < 4; ++j) {
      float s0 = rbf(y8[2 * j]) + rbf(lo16f(cw[j]) * dk);
      float s1 = rbf(y8[2 * j + 1]) + rbf(hi16f(cw[j]) * dk);
      o[j] = pack2(s0, s1);
    }
    const __amdgpu_buffer_rsrc_t sr = __builtin_amdgcn_make_buffer_rsrc((void*)sT, 0, LL * DD * 2, 0x00020000);
    v4u ov = {o[0], o[1], o[2], o[3]};
    __builtin_amdgcn_raw_buffer_store_b128(ov, sr, (unsigned)((d * LL + 8 * t2) * 2), 0, 16);
    if (drain) asm volatile("s_waitcnt vmcnt(0)" ::: "memory");
  }
  __syncthreads();
  STAMP(1, 3);
}

__device__ __forceinline__ void conv_role(int y, const u16* __restrict__ xnT, const u16* __restrict__ Kp,
                                          const u16* __restrict__ Dsk, u16* __restrict__ sT, u32* cnt,
                                          const char* __restrict__ ws, char* smem) {
  const int tid0 = threadIdx.x;
  const int t2 = tid0 & 255;
  const int dA = y, dB = y + 224;
  const uint4* kA = (const uint4*)(Kp + (size_t)dA * 2 * KPAD);
  const uint4* kB = (const uint4*)(Kp + (size_t)dB * 2 * KPAD);
  const uint4* uA = (const uint4*)(xnT + (size_t)dA * LL);
  const uint4* uB = (const uint4*)(xnT + (size_t)dB * LL);
  const int i1 = tid0 < 64 ? tid0 + 512 : tid0, ip = t2 > 0 ? t2 - 1 : 0;
  uint4 a0 = kA[tid0], a1 = kA[i1], ac = uA[t2], ap = uA[ip];
  u16 ad = Dsk[dA];
  uint4 b0 = kB[tid0], b1 = kB[i1], bc = uB[t2], bp = uB[ip];
  u16 bd = Dsk[dB];
  conv_one(dA, a0, a1, ac, ap, ad, false, sT, smem);
  conv_one(dB, b0, b1, bc, bp, bd, true, sT, smem);
  if (tid0 == 0) {
    __hip_atomic_fetch_add(cnt + (16 + (dA >> 6)) * 32, 1u, RLX_AGENT);
    __hip_atomic_fetch_add(cnt + (16 + (dB >> 6)) * 32, 1u, RLX_AGENT);
  }
  STAMP(1, 4);
}

__global__ __launch_bounds__(512, 4) void k2_flow(const u16* __restrict__ b1, const u16* __restrict__ Dsk,
                                                  const u16* __restrict__ x, const u16* __restrict__ b2,
                                                  char* ws, u16* __restrict__ out) {
  __shared__ __attribute__((aligned(16))) char smem[81920];
  const int b = blockIdx.x;
  RSTAMP(1, 14);
  if (b < 224) {
    x_role(b, ws, b1, x, b2, out, smem);
  } else {
    conv_role(b - 224, (const u16*)(ws + WS_XNT), (const u16*)(ws + WS_KP), Dsk, (u16*)(ws + WS_ST),
              (u32*)(ws + WS_CNT), ws, smem);
  }
  RSTAMP(1, 15);
}

extern "C" void kernel_launch(void* const* d_in, const int* in_sizes, int n_in,
                              void* d_out, int out_size, void* d_ws, size_t ws_size,
                              hipStream_t stream) {
  const u16* x = (const u16*)d_in[0];
  const u16* logA = (const u16*)d_in[1];
  const u16* Aim = (const u16*)d_in[2];
  const u16* Cre = (const u16*)d_in[3];
  const u16* Cim = (const u16*)d_in[4];
  const u16* logdt = (const u16*)d_in[5];
  const u16* Dsk = (const u16*)d_in[6];
  const u16* w1 = (const u16*)d_in[8];
  const u16* b1 = (const u16*)d_in[9];
  const u16* w2 = (const u16*)d_in[10];
  const u16* b2 = (const u16*)d_in[11];
  char* ws = (char*)d_ws;
  k1_prep<<<256 + DD, 512, 0, stream>>>(x, logA, Aim, Cre, Cim, logdt, w1, b1, w2, ws);
  k2_flow<<<448, 512, 0, stream>>>(b1, Dsk, x, b2, ws, (u16*)d_out);
}
```

```cpp
#include <hip/hip_runtime.h>
#include <hip/hip_bf16.h>
#include <stdint.h>

typedef __attribute__((ext_vector_type(8))) short bf16x8;
typedef __attribute__((ext_vector_type(4))) float f32x4;
typedef __attribute__((ext_vector_type(16))) float f32x16;
typedef unsigned int u32;
typedef unsigned short u16;

#define LL 2048
#define DD 448
#define NN 256
#define FF 1792
#define KPAD 2304

#define WS_XN 0
#define WS_XNT (WS_XN + LL * DD * 2)
#define WS_W1T (WS_XNT + LL * DD * 2)
#define WS_W2T (WS_W1T + DD * FF * 2)
#define WS_KP (WS_W2T + DD * FF * 2)
#define WS_H (WS_KP + DD * 2 * KPAD * 2)
#define WS_ST (WS_H + LL * FF * 2)
#define WS_CNT (WS_ST + LL * DD * 2)
#define WS_END (WS_CNT + 32 * 128)

#define STAMP(K, I)
#define STAMPV(K, I, V)
#define RSTAMP(K, I)
typedef __attribute__((ext_vector_type(4))) unsigned v4u_;
typedef __attribute__((ext_vector_type(2))) unsigned v2u_;
#define STMODE 2
__device__ __forceinline__ void st16(void* p, uint4 v) {
  asm volatile("global_store_dwordx4 %0, %1, off sc1\n\ts_nop 1" ::"v"(p), "v"((v4u_){v.x, v.y, v.z, v.w}) : "memory");
}
__device__ __forceinline__ void st8(void* p, uint2 v) {
  asm volatile("global_store_dwordx2 %0, %1, off sc1\n\ts_nop 1" ::"v"(p), "v"((v2u_){v.x, v.y}) : "memory");
}
__device__ __forceinline__ float bf2f(u16 v) { return __uint_as_float((u32)v << 16); }
__device__ __forceinline__ u16 f2bf(float f) { __bf16 b = (__bf16)f; return __builtin_bit_cast(u16, b); }
__device__ __forceinline__ float rbf(float f) { return (float)(__bf16)f; }
__device__ __forceinline__ float lo16f(u32 v) { return __uint_as_float(v << 16); }
__device__ __forceinline__ float hi16f(u32 v) { return __uint_as_float(v & 0xffff0000u); }
__device__ __forceinline__ u32 pack2(float a, float b) { return (u32)f2bf(a) | ((u32)f2bf(b) << 16); }

__device__ __forceinline__ float fdiv(float a, float b, float r) {
  float q = a * r;
  float e = fmaf(-q, b, a);
  return fmaf(e, r, q);
}
__device__ __forceinline__ float wave_sum(float v) {
#pragma unroll
  for (int o = 32; o > 0; o >>= 1) v += __shfl_xor(v, o);
  return v;
}

typedef float f2 __attribute__((ext_vector_type(2)));
typedef __bf16 bf2 __attribute__((ext_vector_type(2)));
__device__ __forceinline__ f2 cmul(f2 a, f2 b) {
  f2 t = a.xx * b;
  f2 bs = {-b.y, b.x};
  return __builtin_elementwise_fma(a.yy, bs, t);
}
__device__ __forceinline__ f2 rbf2(f2 v) { return __builtin_convertvector(__builtin_convertvector(v, bf2), f2); }
__device__ __forceinline__ u32 sqrelu2(f2 a, f2 b) {
  f2 v = rbf2(rbf2(a) + b);
  f2 z = {0.f, 0.f};
  v = __builtin_elementwise_max(v, z);
  v = v * v;
  return __builtin_bit_cast(u32, __builtin_convertvector(v, bf2));
}
__device__ __forceinline__ f2 cexp_(float are, float aim) {
  float mag = __builtin_amdgcn_exp2f(are * 1.44269504088896340736f);
  const float I2P_HI = 0.15915493667125702f;
  const float I2P_LO = 6.4206383e-09f;
  float t = aim * I2P_HI;
  float e = fmaf(aim, I2P_HI, -t) + aim * I2P_LO;
  float f = (t - rintf(t)) + e;
  f2 r;
  r.x = mag * __builtin_amdgcn_cosf(f);
  r.y = mag * __builtin_amdgcn_sinf(f);
  return r;
}

#define SB() __builtin_amdgcn_sched_barrier(0)
__device__ __forceinline__ constexpr int ekey(int r) {
  return (int)((r < 16 ? 0x3607495fa3d0afd9ull >> (4 * r) : (r < 32 ? 0x32c4fc79db2a54d0ull >> (4 * (r - 16)) : 0x00000000e72ca570ull >> (4 * (r - 32)))) & 15ull);
}
__device__ __forceinline__ constexpr int gkey(int r) {
  return (int)((r < 16 ? 0x581b76be135f9cd3ull >> (4 * r) : (r < 32 ? 0x52fb08d7941be27dull >> (4 * (r - 16)) : 0x00000000906fb42cull >> (4 * (r - 32)))) & 15ull);
}
__device__ __forceinline__ void kg_store(char* tabn, int swb, int idx, int key, f2 v) {
  bf2 h = __builtin_convertvector(v, bf2);
  f2 hf = __builtin_convertvector(h, f2);
  bf2 l = __builtin_convertvector(v - hf, bf2);
  uint2 w = make_uint2(__builtin_bit_cast(u32, h), __builtin_bit_cast(u32, l));
  *(uint2*)(tabn + idx * 1024 + (swb ^ (key << 4))) = w;
}

#define EK_LO0 0x3607495fa3d0afd9ull
#define EK_LO1 0x000054d0u
#define EK_HI0 0xa57032c4fc79db2aull
#define EK_HI1 0x0000e72cu
#define GK_LO0 0x581b76be135f9cd3ull
#define GK_LO1 0x0000e27du
#define GK_HI0 0xb42c52fb08d7941bull
#define GK_HI1 0x0000906fu
__device__ __forceinline__ void kgen_role(int d, const u16* __restrict__ logA, const u16* __restrict__ Aim,
                                          const u16* __restrict__ Cre, const u16* __restrict__ Cim,
                                          const u16* __restrict__ logdt, u16* __restrict__ Kp,
                                          const u16* __restrict__ w1, const u16* __restrict__ w2,
                                          u16* __restrict__ w1T, u16* __restrict__ w2T, char* smem) {
  const int tid = threadIdx.x;
  const bool has_tr = d < 392;
  const int tt_ = d < 196 ? d : d - 196;
  const u16* tr_in = d < 196 ? w1 : w2;
  u16* tr_out = d < 196 ? w1T : w2T;
  const int trR = d < 196 ? DD : FF, trC = d < 196 ? FF : DD;
  const int tr_r = d < 196 ? tt_ / 28 : tt_ / 7, tr_c = d < 196 ? tt_ % 28 : tt_ % 7;
  uint4 tr_raw = make_uint4(0, 0, 0, 0);
  if (has_tr) tr_raw = *(const uint4*)(tr_in + (size_t)(tr_r * 64 + (tid >> 3)) * trC + tr_c * 64 + (tid & 7) * 8);
  const int wave = tid >> 6, lane = tid & 63;
  const int role = __builtin_amdgcn_readfirstlane(tid >> 7);
  char* tabE = smem;
  char* tabG = smem + 40 * 1024;
  const int nl = tid & 127;
  STAMP(2, 0);
  u16 in_la[2], in_ai[2], in_cr[2], in_ci[2];
  const u16 in_dt = logdt[d];
#pragma unroll
  for (int pass = 0; pass < 2; ++pass) {
    in_la[pass] = logA[d * NN + pass * 128 + nl];
    in_ai[pass] = Aim[d * NN + pass * 128 + nl];
    in_cr[pass] = Cre[d * NN + pass * 128 + nl];
    in_ci[pass] = Cim[d * NN + pass * 128 + nl];
  }

  const float dt = rbf(expf(bf2f(in_dt)));
  STAMPV(2, 1, dt);
  f32x4 acc = {0.f, 0.f, 0.f, 0.f};
  float last = 0.f;
  const int seg = wave & 3, kh = wave >> 2;
  const int fi = lane & 15, fq = lane >> 4;
  int gidx, eidx;
  if (seg == 0) { gidx = fi; eidx = fi; }
  else {
    gidx = 8 + 8 * seg + (fi & 7);
    int e = fi << seg;
    eidx = e < 16 ? e : (e < 32 ? 16 + ((e - 16) >> 1) : (e < 64 ? 24 + ((e - 32) >> 2) : 32 + ((e - 64) >> 3)));
  }
  const int gk = gkey(gidx), ek = ekey(eidx);
  const int swb = ((nl >> 1) << 4) + ((nl & 1) << 3);

#pragma unroll 1
  for (int pass = 0; pass < 2; ++pass) {
    {
      const u16 p_la = pass ? in_la[1] : in_la[0], p_ai = pass ? in_ai[1] : in_ai[0];
      const u16 p_cr = pass ? in_cr[1] : in_cr[0], p_ci = pass ? in_ci[1] : in_ci[0];
      float ea = rbf(expf(bf2f(p_la)));
      if (role < 2) {
        const float are = -ea * dt;
        const float aim = bf2f(p_ai) * dt;
#define PUT(IDX, V) kg_store(tabE, swb, IDX, ekey(IDX), V);
        f2 b2 = cexp_(are * 2.f, aim * 2.f), b4 = cexp_(are * 4.f, aim * 4.f);
        f2 b8 = cexp_(are * 8.f, aim * 8.f), b16 = cexp_(are * 16.f, aim * 16.f);
        f2 b6 = cmul(b4, b2);
        if (role == 0) {
          f2 b0 = {1.f, 0.f};
          f2 b1 = cexp_(are, aim);
          f2 b3 = cmul(b2, b1), b5 = cmul(b4, b1), b7 = cmul(b4, b3);
          PUT(0, b0) PUT(1, b1) PUT(2, b2) PUT(3, b3) PUT(4, b4) PUT(5, b5) PUT(6, b6) PUT(7, b7)
          PUT(8, b8) PUT(9, cmul(b8, b1)) PUT(10, cmul(b8, b2)) PUT(11, cmul(b8, b3)) PUT(12, cmul(b8, b4))
          PUT(13, cmul(b8, b5)) PUT(14, cmul(b8, b6)) PUT(15, cmul(b8, b7))
          PUT(16, b16) PUT(17, cmul(b16, b2)) PUT(18, cmul(b16, b4)) PUT(19, cmul(b16, b6))
        } else {
          f2 b32 = cexp_(are * 32.f, aim * 32.f), b64 = cexp_(are * 64.f, aim * 64.f);
          f2 b10 = cmul(b8, b2), b12 = cmul(b8, b4), b14 = cmul(b8, b6);
          f2 b20 = cmul(b16, b4), b24 = cmul(b16, b8), b28 = cmul(b16, b12);
          PUT(20, b24) PUT(21, cmul(b16, b10)) PUT(22, b28) PUT(23, cmul(b16, b14))
          f2 b40 = cmul(b32, b8), b48 = cmul(b32, b16), b56 = cmul(b32, b24);
          PUT(24, b32) PUT(25, cmul(b32, b4)) PUT(26, b40) PUT(27, cmul(b32, b12)) PUT(28, b48)
          PUT(29, cmul(b32, b20)) PUT(30, b56) PUT(31, cmul(b32, b28))
          PUT(32, b64) PUT(33, cmul(b64, b8)) PUT(34, cmul(b64, b16)) PUT(35, cmul(b64, b24)) PUT(36, cmul(b64, b32))
          PUT(37, cmul(b64, b40)) PUT(38, cmul(b64, b48)) PUT(39, cmul(b64, b56))
        }
#undef PUT
      } else {
        const float are = (-ea * dt) * 16.f;
        const float aim = -(bf2f(p_ai) * dt) * 16.f;
        f2 c;
        c.x = bf2f(p_cr) * dt;
        c.y = -(bf2f(p_ci) * dt);
#define PUT(IDX, V) kg_store(tabG, swb, IDX, gkey(IDX), V);
        f2 b2 = cexp_(are * 2.f, aim * 2.f), b4 = cexp_(are * 4.f, aim * 4.f);
        f2 b8 = cexp_(are * 8.f, aim * 8.f), b16 = cexp_(are * 16.f, aim * 16.f);
        f2 b6 = cmul(b4, b2);
        f2 cb16 = cmul(c, b16);
        if (role == 2) {
          f2 b1 = cexp_(are, aim);
          f2 b3 = cmul(b2, b1), b5 = cmul(b4, b1), b7 = cmul(b4, b3);
          f2 cb8 = cmul(c, b8);
          PUT(0, c) PUT(1, cmul(c, b1)) PUT(2, cmul(c, b2)) PUT(3, cmul(c, b3)) PUT(4, cmul(c, b4))
          PUT(5, cmul(c, b5)) PUT(6, cmul(c, b6)) PUT(7, cmul(c, b7))
          PUT(8, cb8) PUT(9, cmul(cb8, b1)) PUT(10, cmul(cb8, b2)) PUT(11, cmul(cb8, b3)) PUT(12, cmul(cb8, b4))
          PUT(13, cmul(cb8, b5)) PUT(14, cmul(cb8, b6)) PUT(15, cmul(cb8, b7))
          PUT(16, cb16) PUT(17, cmul(cb16, b2)) PUT(18, cmul(cb16, b4)) PUT(19, cmul(cb16, b6))
        } else {
          f2 b32 = cexp_(are * 32.f, aim * 32.f), b64 = cexp_(are * 64.f, aim * 64.f);
          f2 b128 = cexp_(are * 128.f, aim * 128.f);
          last += cmul(c, b128).x;
          f2 cb32 = cmul(c, b32), cb64 = cmul(c, b64);
          f2 b10 = cmul(b8, b2), b12 = cmul(b8, b4), b14 = cmul(b8, b6);
          PUT(20, cmul(cb16, b8)) PUT(21, cmul(cb16, b10)) PUT(22, cmul(cb16, b12)) PUT(23, cmul(cb16, b14))
          f2 b20 = cmul(b16, b4), b24 = cmul(b16, b8), b28 = cmul(b16, b12);
          PUT(24, cb32) PUT(25, cmul(cb32, b4)) PUT(26, cmul(cb32, b8)) PUT(27, cmul(cb32, b12)) PUT(28, cmul(cb32, b16))
          PUT(29, cmul(cb32, b20)) PUT(30, cmul(cb32, b24)) PUT(31, cmul(cb32, b28))
          f2 b40 = cmul(b32, b8), b48 = cmul(b32, b16), b56 = cmul(b32, b24);
          PUT(32, cb64) PUT(33, cmul(cb64, b8)) PUT(34, cmul(cb64, b16)) PUT(35, cmul(cb64, b24)) PUT(36, cmul(cb64, b32))
          PUT(37, cmul(cb64, b40)) PUT(38, cmul(cb64, b48)) PUT(39, cmul(cb64, b56))
        }
#undef PUT
      }
    }
    STAMP(2, 2 + pass * 3);
    __syncthreads();
    STAMP(2, 3 + pass * 3);
    if (pass == 0) STAMP(1, 3);
#pragma unroll
    for (int kb = 0; kb < 2; ++kb) {
      uint4 g[4], e[4];
#pragma unroll
      for (int ks = 0; ks < 4; ++ks) {
        int chunk = (kh * 8 + kb * 4 + ks) * 4 + fq;
        g[ks] = *(const uint4*)(tabG + gidx * 1024 + ((chunk ^ gk) << 4));
        e[ks] = *(const uint4*)(tabE + eidx * 1024 + ((chunk ^ ek) << 4));
      }
      SB();
#pragma unroll
      for (int ks = 0; ks < 4; ++ks) {
        uint4 es = make_uint4(e[ks].y, e[ks].x, e[ks].w, e[ks].z);
        acc = __builtin_amdgcn_mfma_f32_16x16x32_bf16(__builtin_bit_cast(bf16x8, g[ks]), __builtin_bit_cast(bf16x8, es), acc, 0, 0, 0);
        acc = __builtin_amdgcn_mfma_f32_16x16x32_bf16(__builtin_bit_cast(bf16x8, g[ks]), __builtin_bit_cast(bf16x8, e[ks]), acc, 0, 0, 0);
      }
      SB();
    }
    __syncthreads();
    STAMP(2, 4 + pass * 3);
  }
  float* Kd = (float*)(smem + 74752);
  float* red = Kd + 1296;
  last = wave_sum(last);
  if (lane == 0) red[wave] = last;
#pragma unroll
  for (int r = 0; r < 4; ++r) {
    int row = fq * 4 + r;
    if (seg == 0) Kd[kh * 648 + row * 16 + fi] = acc[r];
    else if (row < 8) Kd[kh * 648 + 256 + (seg - 1) * 128 + row * 16 + fi] = acc[r];
  }
  __syncthreads();
  {
    const int g = tid >> 1, hf = tid & 1;
    u32 wh[2], wl[2];
#pragma unroll
    for (int jj = 0; jj < 2; ++jj) {
      f2 kv;
#pragma unroll
      for (int e = 0; e < 2; ++e) {
        int l = 8 * g + 7 - (4 * hf + 2 * jj + e);
        int p = (int)rbf((float)l);
        int idx;
        if (p < 256) idx = p;
        else {
          int s = (31 - __clz(p)) - 7;
          idx = 256 + (s - 1) * 128 + ((p >> s) - 128);
        }
        int ix = idx < 640 ? idx : 0;
        float v = Kd[ix] + Kd[648 + ix];
        if (idx >= 640) v = red[6] + red[7];
        kv[e] = 2.f * v;
      }
      bf2 hq = __builtin_convertvector(kv, bf2);
      bf2 lq = __builtin_convertvector(kv - __builtin_convertvector(hq, f2), bf2);
      wh[jj] = __builtin_bit_cast(u32, hq);
      wl[jj] = __builtin_bit_cast(u32, lq);
    }
    char* ko = (char*)(Kp + (size_t)d * 2 * KPAD);
    st8(ko + 512 + 8 * tid, make_uint2(wh[0], wh[1]));
    st8(ko + 4608 + 512 + 8 * tid, make_uint2(wl[0], wl[1]));
    if (tid < 32) {
      *(uint4*)(ko + 16 * tid) = make_uint4(0, 0, 0, 0);
      *(uint4*)(ko + 4608 + 16 * tid) = make_uint4(0, 0, 0, 0);
    }
  }
  STAMP(2, 8);
  if (has_tr) {
    u16* tile = (u16*)smem;
    u32* tw = (u32*)(tile + (tid >> 3) * 66 + (tid & 7) * 8);
    tw[0] = tr_raw.x; tw[1] = tr_raw.y; tw[2] = tr_raw.z; tw[3] = tr_raw.w;
    __syncthreads();
    const int c = tid >> 3, r8 = tid & 7;
    u32 w[4];
#pragma unroll
    for (int j = 0; j < 4; ++j)
      w[j] = (u32)tile[(r8 * 8 + 2 * j) * 66 + c] | ((u32)tile[(r8 * 8 + 2 * j + 1) * 66 + c] << 16);
    st16(tr_out + (size_t)(tr_c * 64 + c) * trR + tr_r * 64 + r8 * 8, make_uint4(w[0], w[1], w[2], w[3]));
  }
}

__device__ void ln_role(int blk, const u16* __restrict__ x, u16* __restrict__ xn, u16* __restrict__ xnT, char* smem) {
  const int tid = threadIdx.x, wave = tid >> 6, lane = tid & 63;
  u16* tile = (u16*)smem;
  const int row = wave * 8 + (lane >> 3), c = lane & 7;
  const int l = blk * 64 + row;
  STAMP(0, 0);
  uint4 raw[7];
#pragma unroll
  for (int j = 0; j < 7; ++j) raw[j] = *(const uint4*)(x + (size_t)l * DD + j * 64 + c * 8);
  float s = 0.f;
#pragma unroll
  for (int j = 0; j < 7; ++j) {
    s += (lo16f(raw[j].x) + hi16f(raw[j].x)) + (lo16f(raw[j].y) + hi16f(raw[j].y));
    s += (lo16f(raw[j].z) + hi16f(raw[j].z)) + (lo16f(raw[j].w) + hi16f(raw[j].w));
  }
  s += __shfl_xor(s, 1); s += __shfl_xor(s, 2); s += __shfl_xor(s, 4);
  const float mean = s * (1.f / DD);
  STAMP(0, 1);
  float q = 0.f;
#pragma unroll
  for (int j = 0; j < 7; ++j) {
    u32 w4[4] = {raw[j].x, raw[j].y, raw[j].z, raw[j].w};
#pragma unroll
    for (int e = 0; e < 4; ++e) {
      float t0 = lo16f(w4[e]) - mean, t1 = hi16f(w4[e]) - mean;
      q = fmaf(t0, t0, q); q = fmaf(t1, t1, q);
    }
  }
  q += __shfl_xor(q, 1); q += __shfl_xor(q, 2); q += __shfl_xor(q, 4);
  const float var = q * (1.f / DD);
  const float mb = rbf(mean), vb = rbf(var);
  const float den = rbf(sqrtf(rbf(vb + 1e-5f)));
  const float rden = 1.0f / den;
#pragma unroll
  for (int j = 0; j < 7; ++j) {
    u32 w4[4] = {raw[j].x, raw[j].y, raw[j].z, raw[j].w};
    u32 o[4];
#pragma unroll
    for (int e = 0; e < 4; ++e)
      o[e] = pack2(fdiv(rbf(lo16f(w4[e]) - mb), den, rden), fdiv(rbf(hi16f(w4[e]) - mb), den, rden));
    st16(xn + (size_t)l * DD + j * 64 + c * 8, make_uint4(o[0], o[1], o[2], o[3]));
    u32* trow = (u32*)(tile + row * 450 + j * 64 + c * 8);
    trow[0] = o[0]; trow[1] = o[1]; trow[2] = o[2]; trow[3] = o[3];
  }
  STAMP(0, 2);
  __syncthreads();
  STAMP(0, 3);
  for (int v = tid; v < 2 * DD; v += 512) {
    const int d = v >> 1, rh = (v & 1) * 32;
    u32 w[16];
#pragma unroll
    for (int r = 0; r < 16; ++r)
      w[r] = (u32)tile[(rh + 2 * r) * 450 + d] | ((u32)tile[(rh + 2 * r + 1) * 450 + d] << 16);
    uint4* dst = (uint4*)(xnT + (size_t)d * LL + blk * 64 + rh);
    st16(dst, make_uint4(w[0], w[1], w[2], w[3]));
    st16(dst + 1, make_uint4(w[4], w[5], w[6], w[7]));
    st16(dst + 2, make_uint4(w[8], w[9], w[10], w[11]));
    st16(dst + 3, make_uint4(w[12], w[13], w[14], w[15]));
  }
  STAMP(0, 4);
}

__device__ void tr_role(const u16* __restrict__ in, u16* __restrict__ out, int R, int C, int tr, int tc, char* smem) {
  const int tid = threadIdx.x;
  u16* tile = (u16*)smem;
  __syncthreads();
  {
    int v = tid;
    int r = v >> 3, c8 = v & 7;
    uint4 raw = *(const uint4*)(in + (size_t)(tr * 64 + r) * C + tc * 64 + c8 * 8);
    u32* t = (u32*)(tile + r * 66 + c8 * 8);
    t[0] = raw.x; t[1] = raw.y; t[2] = raw.z; t[3] = raw.w;
  }
  __syncthreads();
  {
    int v = tid;
    int c = v >> 3, r8 = v & 7;
    u32 w[4];
#pragma unroll
    for (int j = 0; j < 4; ++j)
      w[j] = (u32)tile[(r8 * 8 + 2 * j) * 66 + c] | ((u32)tile[(r8 * 8 + 2 * j + 1) * 66 + c] << 16);
    *(uint4*)(out + (size_t)(tc * 64 + c) * R + tr * 64 + r8 * 8) = make_uint4(w[0], w[1], w[2], w[3]);
  }
}

__global__ __launch_bounds__(512, 4) void k1_prep(const u16* __restrict__ x, const u16* __restrict__ logA,
                                                  const u16* __restrict__ Aim, const u16* __restrict__ Cre,
                                                  const u16* __restrict__ Cim, const u16* __restrict__ logdt,
                                                  const u16* __restrict__ w1, const u16* __restrict__ w2, char* ws) {
  __shared__ __attribute__((aligned(16))) char smem[81920];
  const int b = blockIdx.x;
  RSTAMP(2, 14);
  if (b < 32) {
    if (b == 0 && threadIdx.x < 32) *(u32*)(ws + WS_CNT + threadIdx.x * 128) = 0u;
    ln_role(b, x, (u16*)(ws + WS_XN), (u16*)(ws + WS_XNT), smem);
  } else {
    kgen_role(b - 32, logA, Aim, Cre, Cim, logdt, (u16*)(ws + WS_KP), w1, w2, (u16*)(ws + WS_W1T),
              (u16*)(ws + WS_W2T), smem);
  }
  RSTAMP(2, 15);
}

#define RLX_AGENT __ATOMIC_RELAXED, __HIP_MEMORY_SCOPE_AGENT
typedef __attribute__((address_space(1))) unsigned gu32;
typedef __attribute__((ext_vector_type(4))) unsigned v4u;

__device__ __forceinline__ void wait_count(u32* word, unsigned want) {
  for (unsigned spins = 0; spins < (1u << 22); ++spins) {
    unsigned v = __builtin_amdgcn_readfirstlane(__hip_atomic_load(word, RLX_AGENT));
    if (v >= want) break;
    __builtin_amdgcn_s_sleep(1);
  }
}

__device__ __forceinline__ void x_role(int blk, char* ws, const u16* __restrict__ b1, const u16* __restrict__ x,
                                       const u16* __restrict__ b2, u16* __restrict__ out, char* smem) {
  const int tid = threadIdx.x, wave = tid >> 6, lane = tid & 63;
  const int xcd = blk & 7, idx = blk >> 3;
  const int r32 = lane & 31, hh = lane >> 5;
  const u16* xn = (const u16*)(ws + WS_XN);
  const u16* w1T = (const u16*)(ws + WS_W1T);
  const u16* w2T = (const u16*)(ws + WS_W2T);
  u16* h = (u16*)(ws + WS_H);
  const u16* sT = (const u16*)(ws + WS_ST);
  u32* cnt = (u32*)(ws + WS_CNT);
  f32x16 acc[2];
  {
    const int mt = xcd * 2 + (idx & 1), nt = idx >> 1;
    const int l0 = mt * 128, f0 = nt * 128;
    const int wr = wave >> 2, wc = wave & 3;
    const u16* srcA;
    const u16* srcB;
    {
      int row = wave * 16 + (lane >> 2);
      int src = (lane & 3) ^ ((row >> 2) & 3);
      srcA = w1T + (size_t)(f0 + row) * DD + src * 8;
      srcB = xn + (size_t)(l0 + row) * DD + src * 8;
    }
    auto gls = [&](int t, int slot, int q) {
      char* dst = smem + slot * 16384 + q * 8192 + wave * 1024 + lane * 16;
      const u16* sp = (q ? srcB : srcA) + t * 32;
      __builtin_amdgcn_global_load_lds((const u32*)sp, (u32*)dst, 16, 0, 0);
    };
    auto gl = [&](int t, int q) { gls(t, t % 5, q); };
    int fo[3], fs[3];
#pragma unroll
    for (int w = 0; w < 3; ++w) {
      int row = (w < 2) ? (wr * 64 + w * 32 + r32) : (wc * 32 + r32);
      fo[w] = (w == 2 ? 8192 : 0) + row * 64;
      fs[w] = (row >> 2) & 3;
    }
#pragma unroll
    for (int i = 0; i < 2; ++i)
#pragma unroll
      for (int r = 0; r < 16; ++r) acc[i][r] = 0.f;
    STAMP(1, 0);
    uint2 braw_[2][4];
#pragma unroll
    for (int i = 0; i < 2; ++i)
#pragma unroll
      for (int g = 0; g < 4; ++g) braw_[i][g] = *(const uint2*)(b1 + f0 + wr * 64 + i * 32 + 8 * g + 4 * hh);
#pragma unroll
    for (int t = 0; t < 4; ++t) { gl(t, 0); gl(t, 1); }
    auto step1 = [&](int t, int slot, int slot4, bool issue) {
      const char* bs = smem + slot * 16384;
      bf16x8 fr[2][3];
#pragma unroll
      for (int kk = 0; kk < 2; ++kk)
#pragma unroll
        for (int w = 0; w < 3; ++w) fr[kk][w] = *(const bf16x8*)(bs + fo[w] + (((2 * kk + hh) ^ fs[w]) << 4));
      SB();
#pragma unroll
      for (int kk = 0; kk < 2; ++kk) {
        if (issue) gls(t + 4, slot4, kk);
        acc[0] = __builtin_amdgcn_mfma_f32_32x32x16_bf16(fr[kk][0], fr[kk][2], acc[0], 0, 0, 0);
        acc[1] = __builtin_amdgcn_mfma_f32_32x32x16_bf16(fr[kk][1], fr[kk][2], acc[1], 0, 0, 0);
        SB();
      }
    };
    {
      int slot = 0;
#pragma unroll 1
      for (int t = 0; t < 10; ++t) {
        asm volatile("s_waitcnt vmcnt(6) lgkmcnt(0)" ::: "memory");
        __builtin_amdgcn_s_barrier();
        step1(t, slot, slot == 0 ? 4 : slot - 1, true);
        slot = slot == 4 ? 0 : slot + 1;
      }
      asm volatile("s_waitcnt vmcnt(6) lgkmcnt(0)" ::: "memory");
      __builtin_amdgcn_s_barrier();
      step1(10, 0, 0, false);
      asm volatile("s_waitcnt vmcnt(4) lgkmcnt(0)" ::: "memory");
      __builtin_amdgcn_s_barrier();
      step1(11, 1, 0, false);
      asm volatile("s_waitcnt vmcnt(2) lgkmcnt(0)" ::: "memory");
      __builtin_amdgcn_s_barrier();
      step1(12, 2, 0, false);
      asm volatile("s_waitcnt vmcnt(0) lgkmcnt(0)" ::: "memory");
      __builtin_amdgcn_s_barrier();
      step1(13, 3, 0, false);
    }
    STAMP(1, 1);
    __syncthreads();
    {
      const int ll = wc * 32 + r32;
#pragma unroll
      for (int i = 0; i < 2; ++i)
#pragma unroll
        for (int g = 0; g < 4; ++g) {
          int fl = wr * 64 + i * 32 + 8 * g + 4 * hh;
          uint2 braw = braw_[i][g];
          f2 bb0 = {lo16f(braw.x), hi16f(braw.x)}, bb1 = {lo16f(braw.y), hi16f(braw.y)};
          f2 a0 = {acc[i][4 * g], acc[i][4 * g + 1]}, a1 = {acc[i][4 * g + 2], acc[i][4 * g + 3]};
          *(uint2*)(smem + ll * 256 + (((fl >> 3) ^ (ll & 15)) << 4) + (fl & 4) * 2) =
              make_uint2(sqrelu2(a0, bb0), sqrelu2(a1, bb1));
        }
    }
    __syncthreads();
    {
      const __amdgpu_buffer_rsrc_t hr = __builtin_amdgcn_make_buffer_rsrc((void*)h, 0, LL * FF * 2, 0x00020000);
#pragma unroll
      for (int i = 0; i < 4; ++i) {
        int row = i * 32 + (tid >> 4), ch = tid & 15;
        v4u v = *(const v4u*)(smem + row * 256 + ((ch ^ (row & 15)) << 4));
        __builtin_amdgcn_raw_buffer_store_b128(v, hr, (unsigned)(((l0 + row) * FF + f0 + ch * 8) * 2), 0, 16);
      }
    }
    asm volatile("s_waitcnt vmcnt(0)" ::: "memory");
    __syncthreads();
    if (tid == 0) __hip_atomic_fetch_add(cnt + mt * 32, 1u, RLX_AGENT);
    STAMP(1, 2);
  }
  {
    const int mt = xcd * 4 + (idx & 3), nt = idx >> 2;
    const int l0 = mt * 64, d0 = nt * 64;
    const int kq = wave & 3, wi = wave >> 2;
    const u16* srcA;
    const u16* srcB;
    {
      int row = wave * 8 + (lane >> 3);
      int src = (lane & 7) ^ ((row >> 1) & 7);
      srcA = w2T + (size_t)(d0 + row) * FF + src * 8;
      srcB = h + (size_t)(l0 + row) * FF + src * 8;
    }
    auto gls = [&](int t, int slot, int q) {
      char* dst = smem + slot * 16384 + q * 8192 + wave * 1024 + lane * 16;
      const u16* sp = (q ? srcB : srcA) + t * 64;
      __builtin_amdgcn_global_load_lds((const u32*)sp, (u32*)dst, 16, 0, 0);
    };
    auto gl = [&](int t, int q) { gls(t, t % 5, q); };
    const int quad = wave >> 1, half = wave & 1;
    const int er = quad >> 1, ec = quad & 1;
    const int l = l0 + ec * 32 + r32;
    uint2 braw_[2], xraw_[2];
#pragma unroll
    for (int g2 = 0; g2 < 2; ++g2) {
      int d = d0 + er * 32 + 8 * (half * 2 + g2) + 4 * hh;
      braw_[g2] = *(const uint2*)(b2 + d);
      xraw_[g2] = *(const uint2*)(x + (size_t)l * DD + d);
    }
#pragma unroll
    for (int t = 0; t < 4; ++t) gl(t, 0);
    if (wave == 0) {
      wait_count(cnt + (mt >> 1) * 32, 14u);
      __builtin_amdgcn_fence(__ATOMIC_ACQUIRE, "agent");
      asm volatile("s_waitcnt vmcnt(0)" ::: "memory");
    }
    __syncthreads();
    STAMP(1, 3);
#pragma unroll
    for (int i = 0; i < 2; ++i)
#pragma unroll
      for (int r = 0; r < 16; ++r) acc[i][r] = 0.f;
#pragma unroll
    for (int t = 0; t < 4; ++t) gl(t, 1);
    int fo[3];
#pragma unroll
    for (int w = 0; w < 3; ++w) {
      int row = (w == 0) ? (wi * 32 + r32) : ((w - 1) * 32 + r32);
      int ch = 2 * kq + hh;
      fo[w] = (w ? 8192 : 0) + row * 128 + ((ch ^ ((row >> 1) & 7)) << 4);
    }
    auto step2 = [&](int t, int slot, int slot4, bool issue) {
      const char* bs = smem + slot * 16384;
      bf16x8 fr[3];
#pragma unroll
      for (int w = 0; w < 3; ++w) fr[w] = *(const bf16x8*)(bs + fo[w]);
      SB();
      if (issue) gls(t + 4, slot4, 0);
      acc[0] = __builtin_amdgcn_mfma_f32_32x32x16_bf16(fr[0], fr[1], acc[0], 0, 0, 0);
      SB();
      if (issue) gls(t + 4, slot4, 1);
      acc[1] = __builtin_amdgcn_mfma_f32_32x32x16_bf16(fr[0], fr[2], acc[1], 0, 0, 0);
      SB();
    };
    {
      asm volatile("s_waitcnt vmcnt(3) lgkmcnt(0)" ::: "memory");
      __builtin_amdgcn_s_barrier();
      step2(0, 0, 4, true);
      asm volatile("s_waitcnt vmcnt(4) lgkmcnt(0)" ::: "memory");
      __builtin_amdgcn_s_barrier();
      step2(1, 1, 0, true);
      asm volatile("s_waitcnt vmcnt(5) lgkmcnt(0)" ::: "memory");
      __builtin_amdgcn_s_barrier();
      step2(2, 2, 1, true);
      int slot = 3;
#pragma unroll 1
      for (int t = 3; t < 24; ++t) {
        asm volatile("s_waitcnt vmcnt(6) lgkmcnt(0)" ::: "memory");
        __builtin_amdgcn_s_barrier();
        step2(t, slot, slot == 0 ? 4 : slot - 1, true);
        slot = slot == 4 ? 0 : slot + 1;
      }
      asm volatile("s_waitcnt vmcnt(6) lgkmcnt(0)" ::: "memory");
      __builtin_amdgcn_s_barrier();
      step2(24, 4, 0, false);
      asm volatile("s_waitcnt vmcnt(4) lgkmcnt(0)" ::: "memory");
      __builtin_amdgcn_s_barrier();
      step2(25, 0, 0, false);
      asm volatile("s_waitcnt vmcnt(2) lgkmcnt(0)" ::: "memory");
      __builtin_amdgcn_s_barrier();
      step2(26, 1, 0, false);
      asm volatile("s_waitcnt vmcnt(0) lgkmcnt(0)" ::: "memory");
      __builtin_amdgcn_s_barrier();
      step2(27, 2, 0, false);
    }
    STAMP(1, 4);
    __syncthreads();
    float* red = (float*)smem;
#pragma unroll
    for (int j = 0; j < 2; ++j)
#pragma unroll
      for (int r4 = 0; r4 < 4; ++r4)
        *(float4*)(red + ((kq * 4 + wi * 2 + j) * 4 + r4) * 256 + lane * 4) =
            make_float4(acc[j][4 * r4], acc[j][4 * r4 + 1], acc[j][4 * r4 + 2], acc[j][4 * r4 + 3]);
    if (wave == 0) wait_count(cnt + (16 + nt) * 32, 64u);
    __syncthreads();
    STAMP(1, 5);
    u16 sraw_[8];
#pragma unroll
    for (int g2 = 0; g2 < 2; ++g2) {
      int d = d0 + er * 32 + 8 * (half * 2 + g2) + 4 * hh;
#pragma unroll
      for (int e = 0; e < 4; ++e) sraw_[4 * g2 + e] = __hip_atomic_load(sT + (size_t)(d + e) * LL + l, RLX_AGENT);
    }
    float fin[8];
#pragma unroll
    for (int r = 0; r < 8; ++r) fin[r] = 0.f;
#pragma unroll
    for (int w = 0; w < 4; ++w)
#pragma unroll
      for (int g2 = 0; g2 < 2; ++g2) {
        float4 v = *(const float4*)(red + ((w * 4 + quad) * 4 + half * 2 + g2) * 256 + lane * 4);
        fin[4 * g2] += v.x; fin[4 * g2 + 1] += v.y; fin[4 * g2 + 2] += v.z; fin[4 * g2 + 3] += v.w;
      }
#pragma unroll
    for (int g2 = 0; g2 < 2; ++g2) {
      int d = d0 + er * 32 + 8 * (half * 2 + g2) + 4 * hh;
      uint2 braw = braw_[g2];
      uint2 xraw = xraw_[g2];
      float bb[4] = {lo16f(braw.x), hi16f(braw.x), lo16f(braw.y), hi16f(braw.y)};
      float xx[4] = {lo16f(xraw.x), hi16f(xraw.x), lo16f(xraw.y), hi16f(xraw.y)};
      float o[4];
#pragma unroll
      for (int e = 0; e < 4; ++e) {
        float v = rbf(rbf(fin[4 * g2 + e]) + bb[e]);
        float gg = rbf(v * __builtin_amdgcn_rcpf(1.f + __builtin_amdgcn_exp2f(v * -1.44269504088896340736f)));
        float s = bf2f(sraw_[4 * g2 + e]);
        o[e] = xx[e] + rbf(s * gg);
      }
      *(uint2*)(out + (size_t)l * DD + d) = make_uint2(pack2(o[0], o[1]), pack2(o[2], o[3]));
    }
    STAMP(1, 6);
  }
}

#define CV_KOFF 0
#define CV_WOFF 9216
#define CV_PHIP 5136
#define CV_ZOFF (CV_WOFF + 8 * CV_PHIP)
#define CV_KD 74752
__device__ __forceinline__ void conv_one(int d, uint4 k0, uint4 k1, uint4 cur, uint4 prv, u16 dkraw, bool drain,
                                         u16* __restrict__ sT, char* smem) {
  int tid = threadIdx.x;
  asm volatile("" : "+v"(tid));
  const int wave = tid >> 6, lane = tid & 63;
  const int t2 = tid & 255;
  STAMP(1, 0);
  if (t2 == 0) prv = make_uint4(0, 0, 0, 0);
  {
    uint4* dst = (uint4*)(smem + CV_KOFF);
    dst[tid] = k0;
    if (tid < 64) dst[tid + 512] = k1;
  }
  if (tid >= 256) {
    u32 Dw[8] = {prv.x, prv.y, prv.z, prv.w, cur.x, cur.y, cur.z, cur.w};
    char* wb = smem + CV_WOFF + (t2 >> 5) * 640 + (t2 & 31) * 16;
#pragma unroll
    for (int phi = 0; phi < 8; ++phi) {
      int e = phi + 1;
      int k = e >> 1;
      uint4 o;
      if ((e & 1) == 0) {
        o = make_uint4(Dw[k], Dw[k + 1], Dw[k + 2], Dw[k + 3]);
      } else {
        o.x = __builtin_amdgcn_alignbit(Dw[k + 1], Dw[k], 16);
        o.y = __builtin_amdgcn_alignbit(Dw[k + 2], Dw[k + 1], 16);
        o.z = __builtin_amdgcn_alignbit(Dw[k + 3], Dw[k + 2], 16);
        o.w = __builtin_amdgcn_alignbit(Dw[k + 4], Dw[k + 3], 16);
      }
      *(uint4*)(wb + phi * CV_PHIP) = o;
    }
    if (t2 == 0) *(uint4*)(smem + CV_ZOFF) = make_uint4(0, 0, 0, 0);
  }
  __syncthreads();
  STAMP(1, 1);
  const int r32 = lane & 31, hh = lane >> 5;
  const int phi = r32 & 7, al = r32 >> 3;
  f32x16 acc0, acc1;
#pragma unroll
  for (int r = 0; r < 16; ++r) { acc0[r] = 0.f; acc1[r] = 0.f; }
  {
    bf16x8 fr[2][6];
    auto rd = [&](int g, int w) -> bf16x8 {
      int dl = g >> 1, tt = g & 1;
      int v = 2 * (wave * 2 + tt) + hh;
      int off;
      if (w < 4) {
        off = CV_KOFF + 512 + 512 * dl + 16 * (r32 - v) + (w & 1) * 4608 + (w >> 1) * 2048;
      } else {
        int ci = al - dl + (w - 4) * 4;
        off = ci < 0 ? CV_ZOFF : (CV_WOFF + phi * CV_PHIP + ci * 640 + v * 16);
      }
      return *(const bf16x8*)(smem + off);
    };
#pragma unroll
    for (int w = 0; w < 6; ++w) fr[0][w] = rd(0, w);
    SB();
#pragma unroll
    for (int g = 0; g < 8; ++g) {
      const int c = g & 1, n = c ^ 1;
      acc0 = __builtin_amdgcn_mfma_f32_32x32x16_bf16(fr[c][1], fr[c][4], acc0, 0, 0, 0);
      if (g < 7) fr[n][4] = rd(g + 1, 4);
      SB();
      acc1 = __builtin_amdgcn_mfma_f32_32x32x16_bf16(fr[c][3], fr[c][4], acc1, 0, 0, 0);
      if (g < 7) fr[n][1] = rd(g + 1, 1);
      SB();
      acc0 = __builtin_amdgcn_mfma_f32_32x32x16_bf16(fr[c][0], fr[c][4], acc0, 0, 0, 0);
      if (g < 7) fr[n][3] = rd(g + 1, 3);
      SB();
      acc1 = __builtin_amdgcn_mfma_f32_32x32x16_bf16(fr[c][2], fr[c][4], acc1, 0, 0, 0);
      if (g < 7) fr[n][0] = rd(g + 1, 0);
      SB();
      acc1 = __builtin_amdgcn_mfma_f32_32x32x16_bf16(fr[c][1], fr[c][5], acc1, 0, 0, 0);
      if (g < 7) fr[n][2] = rd(g + 1, 2);
      SB();
      acc1 = __builtin_amdgcn_mfma_f32_32x32x16_bf16(fr[c][0], fr[c][5], acc1, 0, 0, 0);
      if (g < 7) fr[n][5] = rd(g + 1, 5);
      SB();
    }
  }
  STAMP(1, 2);
  __syncthreads();
  float* Y = (float*)(smem + CV_WOFF);
#pragma unroll
  for (int reg = 0; reg < 16; ++reg) {
    int r = (reg & 3) + 8 * (reg >> 2) + 4 * hh;
    int l = 8 * r + phi + 256 * al;
    Y[wave * 2048 + l] = acc0[reg];
    Y[wave * 2048 + l + 1024] = acc1[reg];
  }
  __syncthreads();
  if (tid >= 256) {
    float y8[8];
#pragma unroll
    for (int j = 0; j < 8; ++j) y8[j] = 0.f;
#pragma unroll
    for (int w = 0; w < 8; ++w) {
      const float4* p = (const float4*)(Y + w * 2048 + 8 * t2);
      float4 a = p[0], b = p[1];
      y8[0] += a.x; y8[1] += a.y; y8[2] += a.z; y8[3] += a.w;
      y8[4] += b.x; y8[5] += b.y; y8[6] += b.z; y8[7] += b.w;
    }
    float dk = bf2f(dkraw);
    u32 cw[4] = {cur.x, cur.y, cur.z, cur.w};
    u32 o[4];
#pragma unroll
    for (int j = 0; j < 4; ++j) {
      float s0 = rbf(y8[2 * j]) + rbf(lo16f(cw[j]) * dk);
      float s1 = rbf(y8[2 * j + 1]) + rbf(hi16f(cw[j]) * dk);
      o[j] = pack2(s0, s1);
    }
    const __amdgpu_buffer_rsrc_t sr = __builtin_amdgcn_make_buffer_rsrc((void*)sT, 0, LL * DD * 2, 0x00020000);
    v4u ov = {o[0], o[1], o[2], o[3]};
    __builtin_amdgcn_raw_buffer_store_b128(ov, sr, (unsigned)((d * LL + 8 * t2) * 2), 0, 16);
    if (drain) asm volatile("s_waitcnt vmcnt(0)" ::: "memory");
  }
  __syncthreads();
  STAMP(1, 3);
}

__device__ __forceinline__ void conv_role(int y, const u16* __restrict__ xnT, const u16* __restrict__ Kp,
                                          const u16* __restrict__ Dsk, u16* __restrict__ sT, u32* cnt,
                                          const char* __restrict__ ws, char* smem) {
  const int tid0 = threadIdx.x;
  const int t2 = tid0 & 255;
  const int dA = y, dB = y + 224;
  const uint4* kA = (const uint4*)(Kp + (size_t)dA * 2 * KPAD);
  const uint4* kB = (const uint4*)(Kp + (size_t)dB * 2 * KPAD);
  const uint4* uA = (const uint4*)(xnT + (size_t)dA * LL);
  const uint4* uB = (const uint4*)(xnT + (size_t)dB * LL);
  const int i1 = tid0 < 64 ? tid0 + 512 : tid0, ip = t2 > 0 ? t2 - 1 : 0;
  uint4 a0 = kA[tid0], a1 = kA[i1], ac = uA[t2], ap = uA[ip];
  u16 ad = Dsk[dA];
  uint4 b0 = kB[tid0], b1 = kB[i1], bc = uB[t2], bp = uB[ip];
  u16 bd = Dsk[dB];
  conv_one(dA, a0, a1, ac, ap, ad, false, sT, smem);
  conv_one(dB, b0, b1, bc, bp, bd, true, sT, smem);
  if (tid0 == 0) {
    __hip_atomic_fetch_add(cnt + (16 + (dA >> 6)) * 32, 1u, RLX_AGENT);
    __hip_atomic_fetch_add(cnt + (16 + (dB >> 6)) * 32, 1u, RLX_AGENT);
  }
  STAMP(1, 4);
}

__global__ __launch_bounds__(512, 4) void k2_flow(const u16* __restrict__ b1, const u16* __restrict__ Dsk,
                                                  const u16* __restrict__ x, const u16* __restrict__ b2,
                                                  char* ws, u16* __restrict__ out) {
  __shared__ __attribute__((aligned(16))) char smem[81920];
  const int b = blockIdx.x;
  RSTAMP(1, 14);
  if (b < 224) {
    x_role(b, ws, b1, x, b2, out, smem);
  } else {
    conv_role(b - 224, (const u16*)(ws + WS_XNT), (const u16*)(ws + WS_KP), Dsk, (u16*)(ws + WS_ST),
              (u32*)(ws + WS_CNT), ws, smem);
  }
  RSTAMP(1, 15);
}

extern "C" void kernel_launch(void* const* d_in, const int* in_sizes, int n_in,
                              void* d_out, int out_size, void* d_ws, size_t ws_size,
                              hipStream_t stream) {
  const u16* x = (const u16*)d_in[0];
  const u16* logA = (const u16*)d_in[1];
  const u16* Aim = (const u16*)d_in[2];
  const u16* Cre = (const u16*)d_in[3];
  const u16* Cim = (const u16*)d_in[4];
  const u16* logdt = (const u16*)d_in[5];
  const u16* Dsk = (const u16*)d_in[6];
  const u16* w1 = (const u16*)d_in[8];
  const u16* b1 = (const u16*)d_in[9];
  const u16* w2 = (const u16*)d_in[10];
  const u16* b2 = (const u16*)d_in[11];
  char* ws = (char*)d_ws;
  k1_prep<<<32 + DD, 512, 0, stream>>>(x, logA, Aim, Cre, Cim, logdt, w1, w2, ws);
  k2_flow<<<448, 512, 0, stream>>>(b1, Dsk, x, b2, ws, (u16*)d_out);
}
```

```cpp
#include <hip/hip_runtime.h>
#include <hip/hip_bf16.h>
#include <stdint.h>

typedef __attribute__((ext_vector_type(8))) short bf16x8;
typedef __attribute__((ext_vector_type(4))) float f32x4;
typedef __attribute__((ext_vector_type(16))) float f32x16;
typedef unsigned int u32;
typedef unsigned short u16;

#define LL 2048
#define DD 448
#define NN 256
#define FF 1792
#define KPAD 2304

#define WS_XN 0
#define WS_XNT (WS_XN + LL * DD * 2)
#define WS_W1T (WS_XNT + LL * DD * 2)
#define WS_W2T (WS_W1T + DD * FF * 2)
#define WS_KP (WS_W2T + DD * FF * 2)
#define WS_H (WS_KP + DD * 2 * KPAD * 2)
#define WS_ST (WS_H + LL * FF * 2)
#define WS_CNT (WS_ST + LL * DD * 2)
#define WS_END (WS_CNT + 32 * 128)

#define STAMP(K, I)
#define STAMPV(K, I, V)
#define RSTAMP(K, I)
typedef __attribute__((ext_vector_type(4))) unsigned v4u_;
typedef __attribute__((ext_vector_type(2))) unsigned v2u_;
#define STMODE 2
__device__ __forceinline__ void st16(void* p, uint4 v) {
  asm volatile("global_store_dwordx4 %0, %1, off sc1\n\ts_nop 1" ::"v"(p), "v"((v4u_){v.x, v.y, v.z, v.w}) : "memory");
}
__device__ __forceinline__ void st8(void* p, uint2 v) {
  asm volatile("global_store_dwordx2 %0, %1, off sc1\n\ts_nop 1" ::"v"(p), "v"((v2u_){v.x, v.y}) : "memory");
}
__device__ __forceinline__ float bf2f(u16 v) { return __uint_as_float((u32)v << 16); }
__device__ __forceinline__ u16 f2bf(float f) { __bf16 b = (__bf16)f; return __builtin_bit_cast(u16, b); }
__device__ __forceinline__ float rbf(float f) { return (float)(__bf16)f; }
__device__ __forceinline__ float lo16f(u32 v) { return __uint_as_float(v << 16); }
__device__ __forceinline__ float hi16f(u32 v) { return __uint_as_float(v & 0xffff0000u); }
__device__ __forceinline__ u32 pack2(float a, float b) { return (u32)f2bf(a) | ((u32)f2bf(b) << 16); }

__device__ __forceinline__ float fdiv(float a, float b, float r) {
  float q = a * r;
  float e = fmaf(-q, b, a);
  return fmaf(e, r, q);
}
__device__ __forceinline__ float wave_sum(float v) {
#pragma unroll
  for (int o = 32; o > 0; o >>= 1) v += __shfl_xor(v, o);
  return v;
}

typedef float f2 __attribute__((ext_vector_type(2)));
typedef __bf16 bf2 __attribute__((ext_vector_type(2)));
__device__ __forceinline__ f2 cmul(f2 a, f2 b) {
  f2 t = a.xx * b;
  f2 bs = {-b.y, b.x};
  return __builtin_elementwise_fma(a.yy, bs, t);
}
__device__ __forceinline__ f2 rbf2(f2 v) { return __builtin_convertvector(__builtin_convertvector(v, bf2), f2); }
__device__ __forceinline__ u32 sqrelu2(f2 a, f2 b) {
  f2 v = rbf2(rbf2(a) + b);
  f2 z = {0.f, 0.f};
  v = __builtin_elementwise_max(v, z);
  v = v * v;
  return __builtin_bit_cast(u32, __builtin_convertvector(v, bf2));
}
__device__ __forceinline__ f2 cexp_(float are, float aim) {
  float mag = __builtin_amdgcn_exp2f(are * 1.44269504088896340736f);
  const float I2P_HI = 0.15915493667125702f;
  const float I2P_LO = 6.4206383e-09f;
  float t = aim * I2P_HI;
  float e = fmaf(aim, I2P_HI, -t) + aim * I2P_LO;
  float f = (t - rintf(t)) + e;
  f2 r;
  r.x = mag * __builtin_amdgcn_cosf(f);
  r.y = mag * __builtin_amdgcn_sinf(f);
  return r;
}

#define SB() __builtin_amdgcn_sched_barrier(0)
__device__ __forceinline__ constexpr int ekey(int r) {
  return (int)((r < 16 ? 0x3607495fa3d0afd9ull >> (4 * r) : (r < 32 ? 0x32c4fc79db2a54d0ull >> (4 * (r - 16)) : 0x00000000e72ca570ull >> (4 * (r - 32)))) & 15ull);
}
__device__ __forceinline__ constexpr int gkey(int r) {
  return (int)((r < 16 ? 0x581b76be135f9cd3ull >> (4 * r) : (r < 32 ? 0x52fb08d7941be27dull >> (4 * (r - 16)) : 0x00000000906fb42cull >> (4 * (r - 32)))) & 15ull);
}
__device__ __forceinline__ void kg_store(char* tabn, int swb, int idx, int key, f2 v) {
  bf2 h = __builtin_convertvector(v, bf2);
  f2 hf = __builtin_convertvector(h, f2);
  bf2 l = __builtin_convertvector(v - hf, bf2);
  uint2 w = make_uint2(__builtin_bit_cast(u32, h), __builtin_bit_cast(u32, l));
  *(uint2*)(tabn + idx * 1024 + (swb ^ (key << 4))) = w;
}

#define EK_LO0 0x3607495fa3d0afd9ull
#define EK_LO1 0x000054d0u
#define EK_HI0 0xa57032c4fc79db2aull
#define EK_HI1 0x0000e72cu
#define GK_LO0 0x581b76be135f9cd3ull
#define GK_LO1 0x0000e27du
#define GK_HI0 0xb42c52fb08d7941bull
#define GK_HI1 0x0000906fu
__device__ __forceinline__ void kgen_role(int d, const u16* __restrict__ logA, const u16* __restrict__ Aim,
                                          const u16* __restrict__ Cre, const u16* __restrict__ Cim,
                                          const u16* __restrict__ logdt, u16* __restrict__ Kp,
                                          const u16* __restrict__ w1, const u16* __restrict__ w2,
                                          u16* __restrict__ w1T, u16* __restrict__ w2T, char* smem) {
  const int tid = threadIdx.x;
  const bool has_tr = d < 392;
  const int tt_ = d < 196 ? d : d - 196;
  const u16* tr_in = d < 196 ? w1 : w2;
  u16* tr_out = d < 196 ? w1T : w2T;
  const int trR = d < 196 ? DD : FF, trC = d < 196 ? FF : DD;
  const int tr_r = d < 196 ? tt_ / 28 : tt_ / 7, tr_c = d < 196 ? tt_ % 28 : tt_ % 7;
  uint4 tr_raw = make_uint4(0, 0, 0, 0);
  if (has_tr) tr_raw = *(const uint4*)(tr_in + (size_t)(tr_r * 64 + (tid >> 3)) * trC + tr_c * 64 + (tid & 7) * 8);
  const int wave = tid >> 6, lane = tid & 63;
  const int role = __builtin_amdgcn_readfirstlane(tid >> 7);
  char* tabE = smem;
  char* tabG = smem + 40 * 1024;
  const int nl = tid & 127;
  STAMP(2, 0);
  u16 in_la[2], in_ai[2], in_cr[2], in_ci[2];
  const u16 in_dt = logdt[d];
#pragma unroll
  for (int pass = 0; pass < 2; ++pass) {
    in_la[pass] = logA[d * NN + pass * 128 + nl];
    in_ai[pass] = Aim[d * NN + pass * 128 + nl];
    in_cr[pass] = Cre[d * NN + pass * 128 + nl];
    in_ci[pass] = Cim[d * NN + pass * 128 + nl];
  }

  const float dt = rbf(expf(bf2f(in_dt)));
  STAMPV(2, 1, dt);
  f32x4 acc = {0.f, 0.f, 0.f, 0.f};
  float last = 0.f;
  const int seg = wave & 3, kh = wave >> 2;
  const int fi = lane & 15, fq = lane >> 4;
  int gidx, eidx;
  if (seg == 0) { gidx = fi; eidx = fi; }
  else {
    gidx = 8 + 8 * seg + (fi & 7);
    int e = fi << seg;
    eidx = e < 16 ? e : (e < 32 ? 16 + ((e - 16) >> 1) : (e < 64 ? 24 + ((e - 32) >> 2) : 32 + ((e - 64) >> 3)));
  }
  const int gk = gkey(gidx), ek = ekey(eidx);
  const int swb = ((nl >> 1) << 4) + ((nl & 1) << 3);

#pragma unroll 1
  for (int pass = 0; pass < 2; ++pass) {
    {
      const u16 p_la = pass ? in_la[1] : in_la[0], p_ai = pass ? in_ai[1] : in_ai[0];
      const u16 p_cr = pass ? in_cr[1] : in_cr[0], p_ci = pass ? in_ci[1] : in_ci[0];
      float ea = rbf(expf(bf2f(p_la)));
      if (role < 2) {
        const float are = -ea * dt;
        const float aim = bf2f(p_ai) * dt;
#define PUT(IDX, V) kg_store(tabE, swb, IDX, ekey(IDX), V);
        f2 b2 = cexp_(are * 2.f, aim * 2.f), b4 = cexp_(are * 4.f, aim * 4.f);
        f2 b8 = cexp_(are * 8.f, aim * 8.f), b16 = cexp_(are * 16.f, aim * 16.f);
        f2 b6 = cmul(b4, b2);
        if (role == 0) {
          f2 b0 = {1.f, 0.f};
          f2 b1 = cexp_(are, aim);
          f2 b3 = cmul(b2, b1), b5 = cmul(b4, b1), b7 = cmul(b4, b3);
          PUT(0, b0) PUT(1, b1) PUT(2, b2) PUT(3, b3) PUT(4, b4) PUT(5, b5) PUT(6, b6) PUT(7, b7)
          PUT(8, b8) PUT(9, cmul(b8, b1)) PUT(10, cmul(b8, b2)) PUT(11, cmul(b8, b3)) PUT(12, cmul(b8, b4))
          PUT(13, cmul(b8, b5)) PUT(14, cmul(b8, b6)) PUT(15, cmul(b8, b7))
          PUT(16, b16) PUT(17, cmul(b16, b2)) PUT(18, cmul(b16, b4)) PUT(19, cmul(b16, b6))
        } else {
          f2 b32 = cexp_(are * 32.f, aim * 32.f), b64 = cexp_(are * 64.f, aim * 64.f);
          f2 b10 = cmul(b8, b2), b12 = cmul(b8, b4), b14 = cmul(b8, b6);
          f2 b20 = cmul(b16, b4), b24 = cmul(b16, b8), b28 = cmul(b16, b12);
          PUT(20, b24) PUT(21, cmul(b16, b10)) PUT(22, b28) PUT(23, cmul(b16, b14))
          f2 b40 = cmul(b32, b8), b48 = cmul(b32, b16), b56 = cmul(b32, b24);
          PUT(24, b32) PUT(25, cmul(b32, b4)) PUT(26, b40) PUT(27, cmul(b32, b12)) PUT(28, b48)
          PUT(29, cmul(b32, b20)) PUT(30, b56) PUT(31, cmul(b32, b28))
          PUT(32, b64) PUT(33, cmul(b64, b8)) PUT(34, cmul(b64, b16)) PUT(35, cmul(b64, b24)) PUT(36, cmul(b64, b32))
          PUT(37, cmul(b64, b40)) PUT(38, cmul(b64, b48)) PUT(39, cmul(b64, b56))
        }
#undef PUT
      } else {
        const float are = (-ea * dt) * 16.f;
        const float aim = -(bf2f(p_ai) * dt) * 16.f;
        f2 c;
        c.x = bf2f(p_cr) * dt;
        c.y = -(bf2f(p_ci) * dt);
#define PUT(IDX, V) kg_store(tabG, swb, IDX, gkey(IDX), V);
        f2 b2 = cexp_(are * 2.f, aim * 2.f), b4 = cexp_(are * 4.f, aim * 4.f);
        f2 b8 = cexp_(are * 8.f, aim * 8.f), b16 = cexp_(are * 16.f, aim * 16.f);
        f2 b6 = cmul(b4, b2);
        f2 cb16 = cmul(c, b16);
        if (role == 2) {
          f2 b1 = cexp_(are, aim);
          f2 b3 = cmul(b2, b1), b5 = cmul(b4, b1), b7 = cmul(b4, b3);
          f2 cb8 = cmul(c, b8);
          PUT(0, c) PUT(1, cmul(c, b1)) PUT(2, cmul(c, b2)) PUT(3, cmul(c, b3)) PUT(4, cmul(c, b4))
          PUT(5, cmul(c, b5)) PUT(6, cmul(c, b6)) PUT(7, cmul(c, b7))
          PUT(8, cb8) PUT(9, cmul(cb8, b1)) PUT(10, cmul(cb8, b2)) PUT(11, cmul(cb8, b3)) PUT(12, cmul(cb8, b4))
          PUT(13, cmul(cb8, b5)) PUT(14, cmul(cb8, b6)) PUT(15, cmul(cb8, b7))
          PUT(16, cb16) PUT(17, cmul(cb16, b2)) PUT(18, cmul(cb16, b4)) PUT(19, cmul(cb16, b6))
        } else {
          f2 b32 = cexp_(are * 32.f, aim * 32.f), b64 = cexp_(are * 64.f, aim * 64.f);
          f2 b128 = cexp_(are * 128.f, aim * 128.f);
          last += cmul(c, b128).x;
          f2 cb32 = cmul(c, b32), cb64 = cmul(c, b64);
          f2 b10 = cmul(b8, b2), b12 = cmul(b8, b4), b14 = cmul(b8, b6);
          PUT(20, cmul(cb16, b8)) PUT(21, cmul(cb16, b10)) PUT(22, cmul(cb16, b12)) PUT(23, cmul(cb16, b14))
          f2 b20 = cmul(b16, b4), b24 = cmul(b16, b8), b28 = cmul(b16, b12);
          PUT(24, cb32) PUT(25, cmul(cb32, b4)) PUT(26, cmul(cb32, b8)) PUT(27, cmul(cb32, b12)) PUT(28, cmul(cb32, b16))
          PUT(29, cmul(cb32, b20)) PUT(30, cmul(cb32, b24)) PUT(31, cmul(cb32, b28))
          f2 b40 = cmul(b32, b8), b48 = cmul(b32, b16), b56 = cmul(b32, b24);
          PUT(32, cb64) PUT(33, cmul(cb64, b8)) PUT(34, cmul(cb64, b16)) PUT(35, cmul(cb64, b24)) PUT(36, cmul(cb64, b32))
          PUT(37, cmul(cb64, b40)) PUT(38, cmul(cb64, b48)) PUT(39, cmul(cb64, b56))
        }
#undef PUT
      }
    }
    STAMP(2, 2 + pass * 3);
    __syncthreads();
    STAMP(2, 3 + pass * 3);
    if (pass == 0) STAMP(1, 3);
#pragma unroll
    for (int kb = 0; kb < 2; ++kb) {
      uint4 g[4], e[4];
#pragma unroll
      for (int ks = 0; ks < 4; ++ks) {
        int chunk = (kh * 8 + kb * 4 + ks) * 4 + fq;
        g[ks] = *(const uint4*)(tabG + gidx * 1024 + ((chunk ^ gk) << 4));
        e[ks] = *(const uint4*)(tabE + eidx * 1024 + ((chunk ^ ek) << 4));
      }
      SB();
#pragma unroll
      for (int ks = 0; ks < 4; ++ks) {
        uint4 es = make_uint4(e[ks].y, e[ks].x, e[ks].w, e[ks].z);
        acc = __builtin_amdgcn_mfma_f32_16x16x32_bf16(__builtin_bit_cast(bf16x8, g[ks]), __builtin_bit_cast(bf16x8, es), acc, 0, 0, 0);
        acc = __builtin_amdgcn_mfma_f32_16x16x32_bf16(__builtin_bit_cast(bf16x8, g[ks]), __builtin_bit_cast(bf16x8, e[ks]), acc, 0, 0, 0);
      }
      SB();
    }
    __syncthreads();
    STAMP(2, 4 + pass * 3);
  }
  float* Kd = (float*)(smem + 74752);
  float* red = Kd + 1296;
  last = wave_sum(last);
  if (lane == 0) red[wave] = last;
#pragma unroll
  for (int r = 0; r < 4; ++r) {
    int row = fq * 4 + r;
    if (seg == 0) Kd[kh * 648 + row * 16 + fi] = acc[r];
    else if (row < 8) Kd[kh * 648 + 256 + (seg - 1) * 128 + row * 16 + fi] = acc[r];
  }
  __syncthreads();
  {
    float* ko = (float*)((char*)Kp + (size_t)d * 2 * KPAD * 2);
    for (int i = tid; i < 641; i += 512) {
      float v = i < 640 ? (Kd[i] + Kd[648 + i]) : (red[6] + red[7]);
      ko[i] = 2.f * v;
    }
  }
  STAMP(2, 8);
  if (has_tr) {
    u16* tile = (u16*)smem;
    u32* tw = (u32*)(tile + (tid >> 3) * 66 + (tid & 7) * 8);
    tw[0] = tr_raw.x; tw[1] = tr_raw.y; tw[2] = tr_raw.z; tw[3] = tr_raw.w;
    __syncthreads();
    const int c = tid >> 3, r8 = tid & 7;
    u32 w[4];
#pragma unroll
    for (int j = 0; j < 4; ++j)
      w[j] = (u32)tile[(r8 * 8 + 2 * j) * 66 + c] | ((u32)tile[(r8 * 8 + 2 * j + 1) * 66 + c] << 16);
    st16(tr_out + (size_t)(tr_c * 64 + c) * trR + tr_r * 64 + r8 * 8, make_uint4(w[0], w[1], w[2], w[3]));
  }
}

__device__ void ln_role(int blk, const u16* __restrict__ x, u16* __restrict__ xn, u16* __restrict__ xnT, char* smem) {
  const int tid = threadIdx.x, wave = tid >> 6, lane = tid & 63;
  u16* tile = (u16*)smem;
  const int row = wave * 8 + (lane >> 3), c = lane & 7;
  const int l = blk * 64 + row;
  STAMP(0, 0);
  uint4 raw[7];
#pragma unroll
  for (int j = 0; j < 7; ++j) raw[j] = *(const uint4*)(x + (size_t)l * DD + j * 64 + c * 8);
  float s = 0.f;
#pragma unroll
  for (int j = 0; j < 7; ++j) {
    s += (lo16f(raw[j].x) + hi16f(raw[j].x)) + (lo16f(raw[j].y) + hi16f(raw[j].y));
    s += (lo16f(raw[j].z) + hi16f(raw[j].z)) + (lo16f(raw[j].w) + hi16f(raw[j].w));
  }
  s += __shfl_xor(s, 1); s += __shfl_xor(s, 2); s += __shfl_xor(s, 4);
  const float mean = s * (1.f / DD);
  STAMP(0, 1);
  float q = 0.f;
#pragma unroll
  for (int j = 0; j < 7; ++j) {
    u32 w4[4] = {raw[j].x, raw[j].y, raw[j].z, raw[j].w};
#pragma unroll
    for (int e = 0; e < 4; ++e) {
      float t0 = lo16f(w4[e]) - mean, t1 = hi16f(w4[e]) - mean;
      q = fmaf(t0, t0, q); q = fmaf(t1, t1, q);
    }
  }
  q += __shfl_xor(q, 1); q += __shfl_xor(q, 2); q += __shfl_xor(q, 4);
  const float var = q * (1.f / DD);
  const float mb = rbf(mean), vb = rbf(var);
  const float den = rbf(sqrtf(rbf(vb + 1e-5f)));
  const float rden = 1.0f / den;
#pragma unroll
  for (int j = 0; j < 7; ++j) {
    u32 w4[4] = {raw[j].x, raw[j].y, raw[j].z, raw[j].w};
    u32 o[4];
#pragma unroll
    for (int e = 0; e < 4; ++e)
      o[e] = pack2(fdiv(rbf(lo16f(w4[e]) - mb), den, rden), fdiv(rbf(hi16f(w4[e]) - mb), den, rden));
    st16(xn + (size_t)l * DD + j * 64 + c * 8, make_uint4(o[0], o[1], o[2], o[3]));
    u32* trow = (u32*)(tile + row * 450 + j * 64 + c * 8);
    trow[0] = o[0]; trow[1] = o[1]; trow[2] = o[2]; trow[3] = o[3];
  }
  STAMP(0, 2);
  __syncthreads();
  STAMP(0, 3);
  for (int v = tid; v < 2 * DD; v += 512) {
    const int d = v >> 1, rh = (v & 1) * 32;
    u32 w[16];
#pragma unroll
    for (int r = 0; r < 16; ++r)
      w[r] = (u32)tile[(rh + 2 * r) * 450 + d] | ((u32)tile[(rh + 2 * r + 1) * 450 + d] << 16);
    uint4* dst = (uint4*)(xnT + (size_t)d * LL + blk * 64 + rh);
    st16(dst, make_uint4(w[0], w[1], w[2], w[3]));
    st16(dst + 1, make_uint4(w[4], w[5], w[6], w[7]));
    st16(dst + 2, make_uint4(w[8], w[9], w[10], w[11]));
    st16(dst + 3, make_uint4(w[12], w[13], w[14], w[15]));
  }
  STAMP(0, 4);
}

__device__ void tr_role(const u16* __restrict__ in, u16* __restrict__ out, int R, int C, int tr, int tc, char* smem) {
  const int tid = threadIdx.x;
  u16* tile = (u16*)smem;
  __syncthreads();
  {
    int v = tid;
    int r = v >> 3, c8 = v & 7;
    uint4 raw = *(const uint4*)(in + (size_t)(tr * 64 + r) * C + tc * 64 + c8 * 8);
    u32* t = (u32*)(tile + r * 66 + c8 * 8);
    t[0] = raw.x; t[1] = raw.y; t[2] = raw.z; t[3] = raw.w;
  }
  __syncthreads();
  {
    int v = tid;
    int c = v >> 3, r8 = v & 7;
    u32 w[4];
#pragma unroll
    for (int j = 0; j < 4; ++j)
      w[j] = (u32)tile[(r8 * 8 + 2 * j) * 66 + c] | ((u32)tile[(r8 * 8 + 2 * j + 1) * 66 + c] << 16);
    *(uint4*)(out + (size_t)(tc * 64 + c) * R + tr * 64 + r8 * 8) = make_uint4(w[0], w[1], w[2], w[3]);
  }
}

__global__ __launch_bounds__(512, 4) void k1_prep(const u16* __restrict__ x, const u16* __restrict__ logA,
                                                  const u16* __restrict__ Aim, const u16* __restrict__ Cre,
                                                  const u16* __restrict__ Cim, const u16* __restrict__ logdt,
                                                  const u16* __restrict__ w1, const u16* __restrict__ w2, char* ws) {
  __shared__ __attribute__((aligned(16))) char smem[81920];
  const int b = blockIdx.x;
  RSTAMP(2, 14);
  if (b < 32) {
    if (b == 0 && threadIdx.x < 32) *(u32*)(ws + WS_CNT + threadIdx.x * 128) = 0u;
    ln_role(b, x, (u16*)(ws + WS_XN), (u16*)(ws + WS_XNT), smem);
  } else {
    kgen_role(b - 32, logA, Aim, Cre, Cim, logdt, (u16*)(ws + WS_KP), w1, w2, (u16*)(ws + WS_W1T),
              (u16*)(ws + WS_W2T), smem);
  }
  RSTAMP(2, 15);
}

#define HAUX 0
#define RLX_AGENT __ATOMIC_RELAXED, __HIP_MEMORY_SCOPE_AGENT
typedef __attribute__((address_space(1))) unsigned gu32;
typedef __attribute__((ext_vector_type(4))) unsigned v4u;

__device__ __forceinline__ void wait_count(u32* word, unsigned want) {
  for (unsigned spins = 0; spins < (1u << 22); ++spins) {
    unsigned v = __builtin_amdgcn_readfirstlane(__hip_atomic_load(word, RLX_AGENT));
    if (v >= want) break;
    __builtin_amdgcn_s_sleep(1);
  }
}

__device__ __forceinline__ void x_role(int blk, char* ws, const u16* __restrict__ b1, const u16* __restrict__ x,
                                       const u16* __restrict__ b2, u16* __restrict__ out, char* smem) {
  const int tid = threadIdx.x, wave = tid >> 6, lane = tid & 63;
  const int xcd = blk & 7, idx = blk >> 3;
  const int r32 = lane & 31, hh = lane >> 5;
  const u16* xn = (const u16*)(ws + WS_XN);
  const u16* w1T = (const u16*)(ws + WS_W1T);
  const u16* w2T = (const u16*)(ws + WS_W2T);
  u16* h = (u16*)(ws + WS_H);
  const u16* sT = (const u16*)(ws + WS_ST);
  u32* cnt = (u32*)(ws + WS_CNT);
  f32x16 acc[2];
  {
    const int mt = xcd * 2 + (idx & 1), nt = idx >> 1;
    const int l0 = mt * 128, f0 = nt * 128;
    const int wr = wave >> 2, wc = wave & 3;
    const u16* srcA;
    const u16* srcB;
    {
      int row = wave * 16 + (lane >> 2);
      int src = (lane & 3) ^ ((row >> 2) & 3);
      srcA = w1T + (size_t)(f0 + row) * DD + src * 8;
      srcB = xn + (size_t)(l0 + row) * DD + src * 8;
    }
    auto gls = [&](int t, int slot, int q) {
      char* dst = smem + slot * 16384 + q * 8192 + wave * 1024 + lane * 16;
      const u16* sp = (q ? srcB : srcA) + t * 32;
      __builtin_amdgcn_global_load_lds((const u32*)sp, (u32*)dst, 16, 0, 0);
    };
    auto gl = [&](int t, int q) { gls(t, t % 5, q); };
    int fo[3], fs[3];
#pragma unroll
    for (int w = 0; w < 3; ++w) {
      int row = (w < 2) ? (wr * 64 + w * 32 + r32) : (wc * 32 + r32);
      fo[w] = (w == 2 ? 8192 : 0) + row * 64;
      fs[w] = (row >> 2) & 3;
    }
#pragma unroll
    for (int i = 0; i < 2; ++i)
#pragma unroll
      for (int r = 0; r < 16; ++r) acc[i][r] = 0.f;
    STAMP(1, 0);
    uint2 braw_[2][4];
#pragma unroll
    for (int i = 0; i < 2; ++i)
#pragma unroll
      for (int g = 0; g < 4; ++g) braw_[i][g] = *(const uint2*)(b1 + f0 + wr * 64 + i * 32 + 8 * g + 4 * hh);
#pragma unroll
    for (int t = 0; t < 4; ++t) { gl(t, 0); gl(t, 1); }
    auto step1 = [&](int t, int slot, int slot4, bool issue) {
      const char* bs = smem + slot * 16384;
      bf16x8 fr[2][3];
#pragma unroll
      for (int kk = 0; kk < 2; ++kk)
#pragma unroll
        for (int w = 0; w < 3; ++w) fr[kk][w] = *(const bf16x8*)(bs + fo[w] + (((2 * kk + hh) ^ fs[w]) << 4));
      SB();
#pragma unroll
      for (int kk = 0; kk < 2; ++kk) {
        if (issue) gls(t + 4, slot4, kk);
        acc[0] = __builtin_amdgcn_mfma_f32_32x32x16_bf16(fr[kk][0], fr[kk][2], acc[0], 0, 0, 0);
        acc[1] = __builtin_amdgcn_mfma_f32_32x32x16_bf16(fr[kk][1], fr[kk][2], acc[1], 0, 0, 0);
        SB();
      }
    };
    {
      int slot = 0;
#pragma unroll 1
      for (int t = 0; t < 10; ++t) {
        asm volatile("s_waitcnt vmcnt(6) lgkmcnt(0)" ::: "memory");
        __builtin_amdgcn_s_barrier();
        step1(t, slot, slot == 0 ? 4 : slot - 1, true);
        slot = slot == 4 ? 0 : slot + 1;
      }
      asm volatile("s_waitcnt vmcnt(6) lgkmcnt(0)" ::: "memory");
      __builtin_amdgcn_s_barrier();
      step1(10, 0, 0, false);
      asm volatile("s_waitcnt vmcnt(4) lgkmcnt(0)" ::: "memory");
      __builtin_amdgcn_s_barrier();
      step1(11, 1, 0, false);
      asm volatile("s_waitcnt vmcnt(2) lgkmcnt(0)" ::: "memory");
      __builtin_amdgcn_s_barrier();
      step1(12, 2, 0, false);
      asm volatile("s_waitcnt vmcnt(0) lgkmcnt(0)" ::: "memory");
      __builtin_amdgcn_s_barrier();
      step1(13, 3, 0, false);
    }
    STAMP(1, 1);
    __syncthreads();
    {
      const int ll = wc * 32 + r32;
#pragma unroll
      for (int i = 0; i < 2; ++i)
#pragma unroll
        for (int g = 0; g < 4; ++g) {
          int fl = wr * 64 + i * 32 + 8 * g + 4 * hh;
          uint2 braw = braw_[i][g];
          f2 bb0 = {lo16f(braw.x), hi16f(braw.x)}, bb1 = {lo16f(braw.y), hi16f(braw.y)};
          f2 a0 = {acc[i][4 * g], acc[i][4 * g + 1]}, a1 = {acc[i][4 * g + 2], acc[i][4 * g + 3]};
          *(uint2*)(smem + ll * 256 + (((fl >> 3) ^ (ll & 15)) << 4) + (fl & 4) * 2) =
              make_uint2(sqrelu2(a0, bb0), sqrelu2(a1, bb1));
        }
    }
    __syncthreads();
    {
      const __amdgpu_buffer_rsrc_t hr = __builtin_amdgcn_make_buffer_rsrc((void*)h, 0, LL * FF * 2, 0x00020000);
#pragma unroll
      for (int i = 0; i < 4; ++i) {
        int row = i * 32 + (tid >> 4), ch = tid & 15;
        v4u v = *(const v4u*)(smem + row * 256 + ((ch ^ (row & 15)) << 4));
        __builtin_amdgcn_raw_buffer_store_b128(v, hr, (unsigned)(((l0 + row) * FF + f0 + ch * 8) * 2), 0, 16);
      }
    }
    asm volatile("s_waitcnt vmcnt(0)" ::: "memory");
    __syncthreads();
    if (tid == 0) __hip_atomic_fetch_add(cnt + mt * 32, 1u, RLX_AGENT);
    STAMP(1, 2);
  }
  {
    const int mt = xcd * 4 + (idx & 3), nt = idx >> 2;
    const int l0 = mt * 64, d0 = nt * 64;
    const int kq = wave & 3, wi = wave >> 2;
    const u16* srcA;
    const u16* srcB;
    {
      int row = wave * 8 + (lane >> 3);
      int src = (lane & 7) ^ ((row >> 1) & 7);
      srcA = w2T + (size_t)(d0 + row) * FF + src * 8;
      srcB = h + (size_t)(l0 + row) * FF + src * 8;
    }
    auto gls = [&](int t, int slot, int q) {
      char* dst = smem + slot * 16384 + q * 8192 + wave * 1024 + lane * 16;
      if (q) __builtin_amdgcn_global_load_lds((const u32*)(srcB + t * 64), (u32*)dst, 16, 0, HAUX);
      else __builtin_amdgcn_global_load_lds((const u32*)(srcA + t * 64), (u32*)dst, 16, 0, 0);
    };
    auto gl = [&](int t, int q) { gls(t, t % 5, q); };
    const int quad = wave >> 1, half = wave & 1;
    const int er = quad >> 1, ec = quad & 1;
    const int l = l0 + ec * 32 + r32;
    uint2 braw_[2], xraw_[2];
#pragma unroll
    for (int g2 = 0; g2 < 2; ++g2) {
      int d = d0 + er * 32 + 8 * (half * 2 + g2) + 4 * hh;
      braw_[g2] = *(const uint2*)(b2 + d);
      xraw_[g2] = *(const uint2*)(x + (size_t)l * DD + d);
    }
#pragma unroll
    for (int t = 0; t < 4; ++t) gl(t, 0);
    if (wave == 0) {
      wait_count(cnt + (mt >> 1) * 32, 14u);
      __builtin_amdgcn_fence(__ATOMIC_ACQUIRE, "agent");
      asm volatile("s_waitcnt vmcnt(0)" ::: "memory");
    }
    __syncthreads();
    STAMP(1, 3);
#pragma unroll
    for (int i = 0; i < 2; ++i)
#pragma unroll
      for (int r = 0; r < 16; ++r) acc[i][r] = 0.f;
#pragma unroll
    for (int t = 0; t < 4; ++t) gl(t, 1);
    int fo[3];
#pragma unroll
    for (int w = 0; w < 3; ++w) {
      int row = (w == 0) ? (wi * 32 + r32) : ((w - 1) * 32 + r32);
      int ch = 2 * kq + hh;
      fo[w] = (w ? 8192 : 0) + row * 128 + ((ch ^ ((row >> 1) & 7)) << 4);
    }
    auto step2 = [&](int t, int slot, int slot4, bool issue) {
      const char* bs = smem + slot * 16384;
      bf16x8 fr[3];
#pragma unroll
      for (int w = 0; w < 3; ++w) fr[w] = *(const bf16x8*)(bs + fo[w]);
      SB();
      if (issue) gls(t + 4, slot4, 0);
      acc[0] = __builtin_amdgcn_mfma_f32_32x32x16_bf16(fr[0], fr[1], acc[0], 0, 0, 0);
      SB();
      if (issue) gls(t + 4, slot4, 1);
      acc[1] = __builtin_amdgcn_mfma_f32_32x32x16_bf16(fr[0], fr[2], acc[1], 0, 0, 0);
      SB();
    };
    {
      asm volatile("s_waitcnt vmcnt(3) lgkmcnt(0)" ::: "memory");
      __builtin_amdgcn_s_barrier();
      step2(0, 0, 4, true);
      asm volatile("s_waitcnt vmcnt(4) lgkmcnt(0)" ::: "memory");
      __builtin_amdgcn_s_barrier();
      step2(1, 1, 0, true);
      asm volatile("s_waitcnt vmcnt(5) lgkmcnt(0)" ::: "memory");
      __builtin_amdgcn_s_barrier();
      step2(2, 2, 1, true);
      int slot = 3;
#pragma unroll 1
      for (int t = 3; t < 24; ++t) {
        asm volatile("s_waitcnt vmcnt(6) lgkmcnt(0)" ::: "memory");
        __builtin_amdgcn_s_barrier();
        step2(t, slot, slot == 0 ? 4 : slot - 1, true);
        slot = slot == 4 ? 0 : slot + 1;
      }
      asm volatile("s_waitcnt vmcnt(6) lgkmcnt(0)" ::: "memory");
      __builtin_amdgcn_s_barrier();
      step2(24, 4, 0, false);
      asm volatile("s_waitcnt vmcnt(4) lgkmcnt(0)" ::: "memory");
      __builtin_amdgcn_s_barrier();
      step2(25, 0, 0, false);
      asm volatile("s_waitcnt vmcnt(2) lgkmcnt(0)" ::: "memory");
      __builtin_amdgcn_s_barrier();
      step2(26, 1, 0, false);
      asm volatile("s_waitcnt vmcnt(0) lgkmcnt(0)" ::: "memory");
      __builtin_amdgcn_s_barrier();
      step2(27, 2, 0, false);
    }
    STAMP(1, 4);
    __syncthreads();
    float* red = (float*)smem;
#pragma unroll
    for (int j = 0; j < 2; ++j)
#pragma unroll
      for (int r4 = 0; r4 < 4; ++r4)
        *(float4*)(red + ((kq * 4 + wi * 2 + j) * 4 + r4) * 256 + lane * 4) =
            make_float4(acc[j][4 * r4], acc[j][4 * r4 + 1], acc[j][4 * r4 + 2], acc[j][4 * r4 + 3]);
    if (wave == 0) wait_count(cnt + (16 + nt) * 32, 64u);
    __syncthreads();
    STAMP(1, 5);
    u16 sraw_[8];
#pragma unroll
    for (int g2 = 0; g2 < 2; ++g2) {
      int d = d0 + er * 32 + 8 * (half * 2 + g2) + 4 * hh;
#pragma unroll
      for (int e = 0; e < 4; ++e) sraw_[4 * g2 + e] = __hip_atomic_load(sT + (size_t)(d + e) * LL + l, RLX_AGENT);
    }
    float fin[8];
#pragma unroll
    for (int r = 0; r < 8; ++r) fin[r] = 0.f;
#pragma unroll
    for (int w = 0; w < 4; ++w)
#pragma unroll
      for (int g2 = 0; g2 < 2; ++g2) {
        float4 v = *(const float4*)(red + ((w * 4 + quad) * 4 + half * 2 + g2) * 256 + lane * 4);
        fin[4 * g2] += v.x; fin[4 * g2 + 1] += v.y; fin[4 * g2 + 2] += v.z; fin[4 * g2 + 3] += v.w;
      }
#pragma unroll
    for (int g2 = 0; g2 < 2; ++g2) {
      int d = d0 + er * 32 + 8 * (half * 2 + g2) + 4 * hh;
      uint2 braw = braw_[g2];
      uint2 xraw = xraw_[g2];
      float bb[4] = {lo16f(braw.x), hi16f(braw.x), lo16f(braw.y), hi16f(braw.y)};
      float xx[4] = {lo16f(xraw.x), hi16f(xraw.x), lo16f(xraw.y), hi16f(xraw.y)};
      float o[4];
#pragma unroll
      for (int e = 0; e < 4; ++e) {
        float v = rbf(rbf(fin[4 * g2 + e]) + bb[e]);
        float gg = rbf(v * __builtin_amdgcn_rcpf(1.f + __builtin_amdgcn_exp2f(v * -1.44269504088896340736f)));
        float s = bf2f(sraw_[4 * g2 + e]);
        o[e] = xx[e] + rbf(s * gg);
      }
      *(uint2*)(out + (size_t)l * DD + d) = make_uint2(pack2(o[0], o[1]), pack2(o[2], o[3]));
    }
    STAMP(1, 6);
  }
}

#define CV_KOFF 0
#define CV_WOFF 9216
#define CV_PHIP 5136
#define CV_ZOFF (CV_WOFF + 8 * CV_PHIP)
#define CV_KD 74752
__device__ __forceinline__ void conv_one(int d, float k0, float k1, uint4 cur, uint4 prv, u16 dkraw, bool drain,
                                         u16* __restrict__ sT, char* smem) {
  int tid = threadIdx.x;
  asm volatile("" : "+v"(tid));
  const int wave = tid >> 6, lane = tid & 63;
  const int t2 = tid & 255;
  STAMP(1, 0);
  if (t2 == 0) prv = make_uint4(0, 0, 0, 0);
  float* Kd = (float*)(smem + CV_KD);
  Kd[tid] = k0;
  if (tid < 129) Kd[512 + tid] = k1;
  if (tid >= 256) {
    u32 Dw[8] = {prv.x, prv.y, prv.z, prv.w, cur.x, cur.y, cur.z, cur.w};
    char* wb = smem + CV_WOFF + (t2 >> 5) * 640 + (t2 & 31) * 16;
#pragma unroll
    for (int phi = 0; phi < 8; ++phi) {
      int e = phi + 1;
      int k = e >> 1;
      uint4 o;
      if ((e & 1) == 0) {
        o = make_uint4(Dw[k], Dw[k + 1], Dw[k + 2], Dw[k + 3]);
      } else {
        o.x = __builtin_amdgcn_alignbit(Dw[k + 1], Dw[k], 16);
        o.y = __builtin_amdgcn_alignbit(Dw[k + 2], Dw[k + 1], 16);
        o.z = __builtin_amdgcn_alignbit(Dw[k + 3], Dw[k + 2], 16);
        o.w = __builtin_amdgcn_alignbit(Dw[k + 4], Dw[k + 3], 16);
      }
      *(uint4*)(wb + phi * CV_PHIP) = o;
    }
    if (t2 == 0) *(uint4*)(smem + CV_ZOFF) = make_uint4(0, 0, 0, 0);
  }
  __syncthreads();
  {
    const int g = tid >> 1, hf = tid & 1;
    u32 wh[2], wl[2];
#pragma unroll
    for (int jj = 0; jj < 2; ++jj) {
      f2 kv;
#pragma unroll
      for (int e = 0; e < 2; ++e) {
        int l = 8 * g + 7 - (4 * hf + 2 * jj + e);
        int p = (int)rbf((float)l);
        int idx;
        if (p < 256) idx = p;
        else {
          int s = (31 - __clz(p)) - 7;
          idx = 256 + (s - 1) * 128 + ((p >> s) - 128);
        }
        kv[e] = Kd[idx];
      }
      bf2 hq = __builtin_convertvector(kv, bf2);
      bf2 lq = __builtin_convertvector(kv - __builtin_convertvector(hq, f2), bf2);
      wh[jj] = __builtin_bit_cast(u32, hq);
      wl[jj] = __builtin_bit_cast(u32, lq);
    }
    char* ko = smem + CV_KOFF;
    *(uint2*)(ko + 512 + 8 * tid) = make_uint2(wh[0], wh[1]);
    *(uint2*)(ko + 4608 + 512 + 8 * tid) = make_uint2(wl[0], wl[1]);
    if (tid < 32) {
      *(uint4*)(ko + 16 * tid) = make_uint4(0, 0, 0, 0);
      *(uint4*)(ko + 4608 + 16 * tid) = make_uint4(0, 0, 0, 0);
    }
  }
  __syncthreads();
  STAMP(1, 1);
  const int r32 = lane & 31, hh = lane >> 5;
  const int phi = r32 & 7, al = r32 >> 3;
  f32x16 acc0, acc1;
#pragma unroll
  for (int r = 0; r < 16; ++r) { acc0[r] = 0.f; acc1[r] = 0.f; }
  {
    bf16x8 fr[2][6];
    auto rd = [&](int g, int w) -> bf16x8 {
      int dl = g >> 1, tt = g & 1;
      int v = 2 * (wave * 2 + tt) + hh;
      int off;
      if (w < 4) {
        off = CV_KOFF + 512 + 512 * dl + 16 * (r32 - v) + (w & 1) * 4608 + (w >> 1) * 2048;
      } else {
        int ci = al - dl + (w - 4) * 4;
        off = ci < 0 ? CV_ZOFF : (CV_WOFF + phi * CV_PHIP + ci * 640 + v * 16);
      }
      return *(const bf16x8*)(smem + off);
    };
#pragma unroll
    for (int w = 0; w < 6; ++w) fr[0][w] = rd(0, w);
    SB();
#pragma unroll
    for (int g = 0; g < 8; ++g) {
      const int c = g & 1, n = c ^ 1;
      acc0 = __builtin_amdgcn_mfma_f32_32x32x16_bf16(fr[c][1], fr[c][4], acc0, 0, 0, 0);
      if (g < 7) fr[n][4] = rd(g + 1, 4);
      SB();
      acc1 = __builtin_amdgcn_mfma_f32_32x32x16_bf16(fr[c][3], fr[c][4], acc1, 0, 0, 0);
      if (g < 7) fr[n][1] = rd(g + 1, 1);
      SB();
      acc0 = __builtin_amdgcn_mfma_f32_32x32x16_bf16(fr[c][0], fr[c][4], acc0, 0, 0, 0);
      if (g < 7) fr[n][3] = rd(g + 1, 3);
      SB();
      acc1 = __builtin_amdgcn_mfma_f32_32x32x16_bf16(fr[c][2], fr[c][4], acc1, 0, 0, 0);
      if (g < 7) fr[n][0] = rd(g + 1, 0);
      SB();
      acc1 = __builtin_amdgcn_mfma_f32_32x32x16_bf16(fr[c][1], fr[c][5], acc1, 0, 0, 0);
      if (g < 7) fr[n][2] = rd(g + 1, 2);
      SB();
      acc1 = __builtin_amdgcn_mfma_f32_32x32x16_bf16(fr[c][0], fr[c][5], acc1, 0, 0, 0);
      if (g < 7) fr[n][5] = rd(g + 1, 5);
      SB();
    }
  }
  STAMP(1, 2);
  __syncthreads();
  float* Y = (float*)(smem + CV_WOFF);
#pragma unroll
  for (int reg = 0; reg < 16; ++reg) {
    int r = (reg & 3) + 8 * (reg >> 2) + 4 * hh;
    int l = 8 * r + phi + 256 * al;
    Y[wave * 2048 + l] = acc0[reg];
    Y[wave * 2048 + l + 1024] = acc1[reg];
  }
  __syncthreads();
  if (tid >= 256) {
    float y8[8];
#pragma unroll
    for (int j = 0; j < 8; ++j) y8[j] = 0.f;
#pragma unroll
    for (int w = 0; w < 8; ++w) {
      const float4* p = (const float4*)(Y + w * 2048 + 8 * t2);
      float4 a = p[0], b = p[1];
      y8[0] += a.x; y8[1] += a.y; y8[2] += a.z; y8[3] += a.w;
      y8[4] += b.x; y8[5] += b.y; y8[6] += b.z; y8[7] += b.w;
    }
    float dk = bf2f(dkraw);
    u32 cw[4] = {cur.x, cur.y, cur.z, cur.w};
    u32 o[4];
#pragma unroll
    for (int j = 0; j < 4; ++j) {
      float s0 = rbf(y8[2 * j]) + rbf(lo16f(cw[j]) * dk);
      float s1 = rbf(y8[2 * j + 1]) + rbf(hi16f(cw[j]) * dk);
      o[j] = pack2(s0, s1);
    }
    const __amdgpu_buffer_rsrc_t sr = __builtin_amdgcn_make_buffer_rsrc((void*)sT, 0, LL * DD * 2, 0x00020000);
    v4u ov = {o[0], o[1], o[2], o[3]};
    __builtin_amdgcn_raw_buffer_store_b128(ov, sr, (unsigned)((d * LL + 8 * t2) * 2), 0, 16);
    if (drain) asm volatile("s_waitcnt vmcnt(0)" ::: "memory");
  }
  __syncthreads();
  STAMP(1, 3);
}

__device__ __forceinline__ void conv_role(int y, const u16* __restrict__ xnT, const u16* __restrict__ Kp,
                                          const u16* __restrict__ Dsk, u16* __restrict__ sT, u32* cnt,
                                          const char* __restrict__ ws, char* smem) {
  const int tid0 = threadIdx.x;
  const int t2 = tid0 & 255;
  const int dA = y, dB = y + 224;
  const float* kA = (const float*)((const char*)Kp + (size_t)dA * 2 * KPAD * 2);
  const float* kB = (const float*)((const char*)Kp + (size_t)dB * 2 * KPAD * 2);
  const uint4* uA = (const uint4*)(xnT + (size_t)dA * LL);
  const uint4* uB = (const uint4*)(xnT + (size_t)dB * LL);
  const int i1 = tid0 < 129 ? tid0 + 512 : tid0, ip = t2 > 0 ? t2 - 1 : 0;
  float a0 = kA[tid0], a1 = kA[i1];
  uint4 ac = uA[t2], ap = uA[ip];
  u16 ad = Dsk[dA];
  float b0 = kB[tid0], b1 = kB[i1];
  uint4 bc = uB[t2], bp = uB[ip];
  u16 bd = Dsk[dB];
  conv_one(dA, a0, a1, ac, ap, ad, false, sT, smem);
  conv_one(dB, b0, b1, bc, bp, bd, true, sT, smem);
  if (tid0 == 0) {
    __hip_atomic_fetch_add(cnt + (16 + (dA >> 6)) * 32, 1u, RLX_AGENT);
    __hip_atomic_fetch_add(cnt + (16 + (dB >> 6)) * 32, 1u, RLX_AGENT);
  }
  STAMP(1, 4);
}

__global__ __launch_bounds__(512, 4) void k2_flow(const u16* __restrict__ b1, const u16* __restrict__ Dsk,
                                                  const u16* __restrict__ x, const u16* __restrict__ b2,
                                                  char* ws, u16* __restrict__ out) {
  __shared__ __attribute__((aligned(16))) char smem[81920];
  const int b = blockIdx.x;
  RSTAMP(1, 14);
  if (b < 224) {
    x_role(b, ws, b1, x, b2, out, smem);
  } else {
    conv_role(b - 224, (const u16*)(ws + WS_XNT), (const u16*)(ws + WS_KP), Dsk, (u16*)(ws + WS_ST),
              (u32*)(ws + WS_CNT), ws, smem);
  }
  RSTAMP(1, 15);
}

extern "C" void kernel_launch(void* const* d_in, const int* in_sizes, int n_in,
                              void* d_out, int out_size, void* d_ws, size_t ws_size,
                              hipStream_t stream) {
  const u16* x = (const u16*)d_in[0];
  const u16* logA = (const u16*)d_in[1];
  const u16* Aim = (const u16*)d_in[2];
  const u16* Cre = (const u16*)d_in[3];
  const u16* Cim = (const u16*)d_in[4];
  const u16* logdt = (const u16*)d_in[5];
  const u16* Dsk = (const u16*)d_in[6];
  const u16* w1 = (const u16*)d_in[8];
  const u16* b1 = (const u16*)d_in[9];
  const u16* w2 = (const u16*)d_in[10];
  const u16* b2 = (const u16*)d_in[11];
  char* ws = (char*)d_ws;
  k1_prep<<<32 + DD, 512, 0, stream>>>(x, logA, Aim, Cre, Cim, logdt, w1, w2, ws);
  k2_flow<<<448, 512, 0, stream>>>(b1, Dsk, x, b2, ws, (u16*)d_out);
}
```
